```python
import torch

def _softmax(x, axis=-1):
    return torch.softmax(x, dim=axis)
def _gelu(x):
    return torch.nn.functional.gelu(x, approximate="tanh")
def _silu(x):
    return torch.nn.functional.silu(x)
def _sigmoid(x):
    return torch.sigmoid(x)
def _layernorm(x, axis=-1, eps=1e-5):
    m = x.mean(dim=axis, keepdim=True)
    v = x.var(dim=axis, keepdim=True, unbiased=False)
    return (x - m) / torch.sqrt(v + eps)
def _scatter_add(tgt, idx, src, axis=0):
    return tgt.clone().index_add_(axis, idx, src)
def _segment_sum(src, seg, n):
    out = torch.zeros((n,) + src.shape[1:], dtype=src.dtype)
    return out.index_add_(0, seg, src)
def _pad(x, pw):
    flat = tuple(v for pair in reversed(pw) for v in pair)
    return torch.nn.functional.pad(x, flat)

kernel_name = 'concat_heads__spectral_fnet__sliding_local_dilated__prompt'

def setup_inputs():

    _w = (lambda a: (lambda t: t.to(torch.bfloat16) if t.is_floating_point() else t)(torch.as_tensor(a)))
    def _A_setup():
        B, S, D = 1, 2048, 512
        ns = torch.arange(S)[:, None] * torch.arange(S)[None, :]
        nd = torch.arange(D)[:, None] * torch.arange(D)[None, :]
        Ws_re = torch.cos(-2.0 * torch.pi * ns / S).to(float)
        Ws_im = torch.sin(-2.0 * torch.pi * ns / S).to(float)
        Wd_re = torch.cos(-2.0 * torch.pi * nd / D).to(float)
        Wd_im = torch.sin(-2.0 * torch.pi * nd / D).to(float)
        return {
            "x": _w((lambda *s: torch.randn(*s, dtype=torch.bfloat16) * 0.05)(B, S, D)),
            "dft_seq_re": _w(Ws_re),
            "dft_seq_im": _w(Ws_im),
            "dft_hid_re": _w(Wd_re),
            "dft_hid_im": _w(Wd_im),
        }
    _A_in = _A_setup()

    def _B_setup():
        B, S, D = 1, 2048, 512
        return {
            "x": _w((lambda *s: torch.randn(*s, dtype=torch.bfloat16) * 0.2)(B, S, D)),
            "wqkv": _w((lambda *s: torch.randn(*s, dtype=torch.bfloat16) * 0.8)(D, 3 * D)),
            "wo": _w((lambda *s: torch.randn(*s, dtype=torch.bfloat16) * 0.2)(D, D)),
        }
    _B_in = _B_setup()

    return {
        "A_x": _A_in["x"],
        "A_dft_seq_re": _A_in["dft_seq_re"],
        "A_dft_seq_im": _A_in["dft_seq_im"],
        "A_dft_hid_re": _A_in["dft_hid_re"],
        "A_dft_hid_im": _A_in["dft_hid_im"],
        "B_x": _B_in["x"],
        "B_wqkv": _B_in["wqkv"],
        "B_wo": _B_in["wo"],
    }

def reference(A_x, A_dft_seq_re, A_dft_seq_im, A_dft_hid_re, A_dft_hid_im, B_x, B_wqkv, B_wo):
    def _A_ref(x, dft_seq_re, dft_seq_im, dft_hid_re, dft_hid_im):
        a = torch.einsum("bsd,ed->bse", x, dft_hid_re)
        b = torch.einsum("bsd,ed->bse", x, dft_hid_im)
        y = torch.einsum("ts,bse->bte", dft_seq_re, a) - torch.einsum("ts,bse->bte", dft_seq_im, b)
        return y

    def _B_ref(x, wqkv, wo):
        B, S, D = x.shape
        H = 32; Dh = D // H
        W, dil = 8, 2
        qkv = (lambda x, *d: x.permute(*d))((x @ wqkv).reshape(B, S, 3, H, Dh), 2, 0, 3, 1, 4)
        q, k, v = qkv[0], qkv[1], qkv[2]
        scale = Dh ** -0.5
        pos = torch.arange(S)
        off = torch.arange(W) * dil
        idx = pos[:, None] - off[None, :]
        mask = idx >= 0
        idx_c = torch.where(mask, idx, 0).reshape(-1)
        kg = k[:, :, idx_c, :].reshape(B, H, S, W, Dh)
        vg = v[:, :, idx_c, :].reshape(B, H, S, W, Dh)
        logits = torch.einsum("bhsd,bhswd->bhsw", q, kg) * scale
        logits = torch.where(mask[None, None, :, :], logits, -1e9)
        attn = _softmax(logits, axis=-1)
        o = torch.einsum("bhsw,bhswd->bhsd", attn, vg)
        return (lambda x, *d: x.permute(*d))(o, 0, 2, 1, 3).reshape(B, S, D) @ wo

    a = _A_ref(A_x, A_dft_seq_re, A_dft_seq_im, A_dft_hid_re, A_dft_hid_im)
    b = _B_ref(B_x, B_wqkv, B_wo)
    return torch.concatenate([a, b], axis=-1)
```

```cpp
#include <hip/hip_runtime.h>
#include <hip/hip_bf16.h>
typedef __hip_bfloat16 bf16;
__device__ __forceinline__ float b2f(bf16 x){ return __bfloat162float(x);}
__device__ __forceinline__ bf16 f2b(float x){ return __float2bfloat16(x);}
__device__ __forceinline__ float rb(float x){ return __bfloat162float(__float2bfloat16(x));}
template<bool BT>
__global__ void gemm_naive(const bf16* A,int lda,const bf16* B,int ldb,bf16* C,int ldc,int M,int N,int K){
  int n=blockIdx.x*64+threadIdx.x%64, m=blockIdx.y*4+threadIdx.x/64;
  float s=0;
  for(int k=0;k<K;++k) s+=b2f(A[(long)m*lda+k])*b2f(BT?B[(long)n*ldb+k]:B[(long)k*ldb+n]);
  C[(long)m*ldc+n]=f2b(s);
}
__global__ void ygemm(const bf16* P,const bf16* Q,const bf16* Wr,const bf16* Wi,bf16* out){
  int n=blockIdx.x*64+threadIdx.x%64, m=blockIdx.y*4+threadIdx.x/64;
  float s=0;
  for(int k=0;k<512;++k) s+=b2f(P[m*512+k])*b2f(Wr[n*512+k])-b2f(Q[m*512+k])*b2f(Wi[n*512+k]);
  out[m*1024+n]=f2b(s);
}
__global__ void attn(const bf16* qkv,bf16* o){
  int idx=blockIdx.x*256+threadIdx.x;
  int s=idx/32,h=idx%32;
  float q[16]; for(int d=0;d<16;++d) q[d]=b2f(qkv[s*1536+h*16+d]);
  float l[8]; float mx=-1e30f;
  for(int w=0;w<8;++w){int j=s-2*w; float a=0; if(j>=0){for(int d=0;d<16;++d)a+=q[d]*b2f(qkv[j*1536+512+h*16+d]); a=rb(rb(a)*0.25f);} else a=rb(-1e9f); l[w]=a; mx=fmaxf(mx,a);}
  float sum=0; for(int w=0;w<8;++w){l[w]=__expf(l[w]-mx); sum+=l[w];}
  float acc[16]; for(int d=0;d<16;++d)acc[d]=0;
  for(int w=0;w<8;++w){int j=s-2*w; if(j<0)j=0; float p=rb(l[w]/sum); for(int d=0;d<16;++d)acc[d]+=p*b2f(qkv[j*1536+1024+h*16+d]);}
  for(int d=0;d<16;++d)o[s*512+h*16+d]=f2b(acc[d]);
}
extern "C" void kernel_launch(void* const* d_in, const int* in_sizes, int n_in,
                              void* d_out, int out_size, void* d_ws, size_t ws_size,
                              hipStream_t stream) {
  const bf16* xA=(const bf16*)d_in[0]; const bf16* Sr=(const bf16*)d_in[1]; const bf16* Si=(const bf16*)d_in[2];
  const bf16* Dr=(const bf16*)d_in[3]; const bf16* Di=(const bf16*)d_in[4]; const bf16* xB=(const bf16*)d_in[5];
  const bf16* wqkv=(const bf16*)d_in[6]; const bf16* wo=(const bf16*)d_in[7];
  bf16* out=(bf16*)d_out; bf16* ws=(bf16*)d_ws;
  bf16* P=ws; bf16* Q=ws+2048*512; bf16* qkv=ws+2*2048*512; bf16* o=qkv+2048*1536;
  gemm_naive<false><<<dim3(8,512),256,0,stream>>>(Sr,2048,xA,512,P,512,2048,512,2048);
  gemm_naive<false><<<dim3(8,512),256,0,stream>>>(Si,2048,xA,512,Q,512,2048,512,2048);
  gemm_naive<false><<<dim3(24,512),256,0,stream>>>(xB,512,wqkv,1536,qkv,1536,2048,1536,512);
  attn<<<256,256,0,stream>>>(qkv,o);
  ygemm<<<dim3(8,512),256,0,stream>>>(P,Q,Dr,Di,out);
  gemm_naive<false><<<dim3(8,512),256,0,stream>>>(o,512,wo,512,out+512,1024,2048,512,512);
}
```

```cpp
#include <hip/hip_runtime.h>
#include <hip/hip_bf16.h>
#include <utility>

typedef __attribute__((ext_vector_type(8))) short bf16x8;
typedef __attribute__((ext_vector_type(4))) short bf16x4;
typedef __attribute__((ext_vector_type(4))) float f32x4;
typedef __attribute__((ext_vector_type(2))) float f32x2;
typedef __attribute__((ext_vector_type(2))) __bf16 bf2;
typedef __attribute__((ext_vector_type(4))) unsigned u32x4;
typedef __attribute__((ext_vector_type(2))) unsigned u32x2;
typedef __amdgpu_buffer_rsrc_t rsrc_t;
#define LAS __attribute__((address_space(3)))
#define DEV __device__ __forceinline__

#define WAIT_V(n) asm volatile("s_waitcnt vmcnt(%0)" ::"n"(n) : "memory")
#define WAIT_L0() asm volatile("s_waitcnt lgkmcnt(0)" ::: "memory")
#define BAR() do{asm volatile("" ::: "memory"); __builtin_amdgcn_s_barrier(); asm volatile("" ::: "memory");}while(0)
#define SCHED0() __builtin_amdgcn_sched_barrier(0)

constexpr int SLOT = 32768, NSLOT = 4;
constexpr unsigned WS_AB = 0, WS_O = 4u << 20, WS_PART = 8u << 20;

#define AUX_S 0

template <int... I, class F> DEV void static_for_impl(std::integer_sequence<int, I...>, F&& f) { (f(std::integral_constant<int, I>{}), ...); }
template <int N, class F> DEV void static_for(F&& f) { static_for_impl(std::make_integer_sequence<int, N>{}, f); }

template <int AUX> DEV void dma16(rsrc_t r, unsigned voff, unsigned soff, char* lds) {
  __builtin_amdgcn_raw_ptr_buffer_load_lds(r, (LAS void*)lds, 16, voff, soff, 0, AUX);
}
DEV rsrc_t mk(const void* p, unsigned bytes) { return __builtin_amdgcn_make_buffer_rsrc((void*)p, 0, bytes, 0x00020000); }
DEV unsigned pk(float a, float b) { f32x2 v = {a, b}; return __builtin_bit_cast(unsigned, __builtin_convertvector(v, bf2)); }
DEV bf16x4 pk4(f32x4 v) { u32x2 r = {pk(v[0], v[1]), pk(v[2], v[3])}; return __builtin_bit_cast(bf16x4, r); }
DEV bf16x8 lds128(const char* p) { return *(const bf16x8*)p; }
DEV bf16x4 ldstr(const char* p) { return __builtin_amdgcn_ds_read_tr16_b64_v4i16((LAS bf16x4*)p); }
DEV bf16x8 cat(bf16x4 a, bf16x4 b) { return __builtin_shufflevector(a, b, 0, 1, 2, 3, 4, 5, 6, 7); }
DEV f32x4 mma(bf16x8 a, bf16x8 b, f32x4 c) { return __builtin_amdgcn_mfma_f32_16x16x32_bf16(a, b, c, 0, 0, 0); }
DEV f32x4 mma16(bf16x4 a, bf16x4 b, f32x4 c) { return __builtin_amdgcn_mfma_f32_16x16x16bf16_1k(a, b, c, 0, 0, 0); }
DEV float rbf(float x) { return __builtin_bit_cast(float, pk(x, x) << 16); }
DEV unsigned opq(unsigned u) { asm volatile("" : "+v"(u)); return u; }
DEV float red4max(float x) {
  unsigned u = __builtin_bit_cast(unsigned, x);
  auto a = __builtin_amdgcn_permlane32_swap(u, opq(u), false, false);
  unsigned a0 = a[0], a1 = a[1];
  x = fmaxf(__builtin_bit_cast(float, a0), __builtin_bit_cast(float, a1));
  u = __builtin_bit_cast(unsigned, x);
  auto b = __builtin_amdgcn_permlane16_swap(u, opq(u), false, false);
  unsigned b0 = b[0], b1 = b[1];
  return fmaxf(__builtin_bit_cast(float, b0), __builtin_bit_cast(float, b1));
}
DEV float red4sum(float x) {
  unsigned u = __builtin_bit_cast(unsigned, x);
  auto a = __builtin_amdgcn_permlane32_swap(u, opq(u), false, false);
  unsigned a0 = a[0], a1 = a[1];
  x = __builtin_bit_cast(float, a0) + __builtin_bit_cast(float, a1);
  u = __builtin_bit_cast(unsigned, x);
  auto b = __builtin_amdgcn_permlane16_swap(u, opq(u), false, false);
  unsigned b0 = b[0], b1 = b[1];
  return __builtin_bit_cast(float, b0) + __builtin_bit_cast(float, b1);
}
DEV u32x4 swap16(u32x2 x, u32x2 y) {
  auto a = __builtin_amdgcn_permlane16_swap(x[0], y[0], false, false);
  auto b = __builtin_amdgcn_permlane16_swap(x[1], y[1], false, false);
  u32x4 r = {a[0], b[0], a[1], b[1]};
  return r;
}

template <int PH> struct Sched {
  static constexpr int n0 = (PH & 1) ? 8 : 0, n1 = (PH & 2) ? 8 : 0, n2 = (PH & 4) ? 16 : 0, n3 = (PH & 8) ? 4 : 0;
  static constexpr int G = n0 + n1 + n2 + n3;
  static constexpr int phase(int g) { return g < n0 ? 0 : g < n0 + n1 ? 1 : g < n0 + n1 + n2 ? 2 : g < G ? 3 : 4; }
  static constexpr int idx(int g) { return g < n0 ? g : g < n0 + n1 ? g - n0 : g < n0 + n1 + n2 ? g - n0 - n1 : g - n0 - n1 - n2; }
  static constexpr int cnt(int role, int g) {
    int p = phase(g);
    return p == 0 ? (role ? 4 : 8) : p == 1 ? (role ? 6 : 9) : p == 4 ? 0 : 8;
  }
};

template <int PH>
__global__ __launch_bounds__(512, 2) void fused(const char* __restrict__ xA, const char* __restrict__ Sr, const char* __restrict__ Si,
                                                const char* __restrict__ Dr, const char* __restrict__ Di, const char* __restrict__ xB,
                                                const char* __restrict__ wqkv, const char* __restrict__ wo, char* __restrict__ out,
                                                char* __restrict__ ws) {
  extern __shared__ __attribute__((aligned(16))) char shm[];
  using SC = Sched<PH>;
  const int bid = blockIdx.x, xcd = bid & 7, loc = bid >> 3;
  const int wid = __builtin_amdgcn_readfirstlane(threadIdx.x >> 6), lane = threadIdx.x & 63;
  const int ab_s = 4 * (xcd & 3) + (loc & 3), ab_e = 8 * (xcd >> 2) + (loc >> 2);
  const int q_j = 4 * (xcd & 3) + (loc & 3), q_hp = 8 * (xcd >> 2) + (loc >> 2);
  const int b_kq = xcd & 3, b_mt = 8 * (xcd >> 2) + (loc >> 2), b_nt = loc & 3;
  const int o_pb = 8 * (xcd & 3) + (loc & 7), o_nt = 4 * (xcd >> 2) + (loc >> 3);

  if (wid >= 4) {
    __builtin_amdgcn_s_setprio(3);
    const int lw = wid & 1;
    const int r = lane >> 3, p = lane & 7;
    if (wid < 6) {
      rsrc_t rxA = mk(xA, 2u << 20), rxB = mk(xB, 2u << 20), rS = mk((b_kq >> 1) ? Si : Sr, 8u << 20), rWo = mk(wo, 512u << 10);
      const unsigned v_ab = r * 1024 + ((p ^ ((r >> 1) & 1)) * 16);
      const unsigned v_q = r * 2048 + ((p ^ (r >> 1)) * 16);
      const unsigned v_s = r * 4096 + ((p ^ (r >> 1)) * 16);
      const unsigned v_w = r * 1024 + ((p ^ (((r >> 1) & 1) * 2)) * 16);
      const unsigned s_ab = (ab_s * 128 + lw * 64) * 1024;
      const int q_row0 = ((q_j & 7) * 128 - 16) * 2 + (q_j >> 3);
      const unsigned s_s = (b_mt * 128 + lw * 64) * 4096 + (b_kq & 1) * 2048;
      const unsigned s_w = (lw * 64) * 1024 + o_nt * 128;
      auto issue = [&](auto gc) {
        constexpr int g = gc; constexpr int ph = SC::phase(g), k = SC::idx(g);
        char* l = shm + (g % NSLOT) * SLOT;
        if constexpr (ph == 0) {
#pragma unroll
          for (int i = 0; i < 8; ++i) dma16<0>(rxA, v_ab ^ ((i & 3) << 5), s_ab + i * 8192 + k * 128, l + (lw * 8 + i) * 1024);
        } else if constexpr (ph == 1) {
#pragma unroll
          for (int i = 0; i < 9; ++i) {
            int ii = lw * 9 + i;
            int row = q_row0 + ii * 16; if (row < 0) row += 32;
            dma16<0>(rxB, v_q ^ ((ii & 1) << 6), row * 1024 + k * 128, l + ii * 1024);
          }
        } else if constexpr (ph == 2) {
#pragma unroll
          for (int i = 0; i < 8; ++i) dma16<AUX_S>(rS, v_s ^ ((i & 1) << 6), s_s + i * 32768 + k * 128, l + (lw * 8 + i) * 1024);
        } else if constexpr (ph == 3) {
#pragma unroll
          for (int i = 0; i < 8; ++i) dma16<0>(rWo, v_w ^ ((i & 1) << 6), s_w + i * 8192 + k * 131072, l + (lw * 8 + i) * 1024);
        }
      };
      static_for<NSLOT - 1>([&](auto gc) { issue(gc); });
      static_for<SC::G>([&](auto gc) {
        constexpr int g = gc;
        WAIT_V(SC::cnt(0, g + 1) + SC::cnt(0, g + 2));
        BAR();
        issue(std::integral_constant<int, g + NSLOT - 1>{});
      });
    } else {
      rsrc_t rD = mk((ab_e >> 3) ? Di : Dr, 512u << 10), rW = mk(wqkv, 1536u << 10), rAB = mk(ws + WS_AB, 4u << 20), rO = mk(ws + WS_O, 2u << 20);
      const unsigned v_ab = r * 1024 + ((p ^ ((r >> 1) & 1)) * 16);
      unsigned v_q[3];
#pragma unroll
      for (int i = 0; i < 3; ++i) {
        int idx = 64 * i + lane, row = idx / 12, cc = idx % 12;
        v_q[i] = row * 3072 + (cc >> 2) * 1024 + (((cc & 3) ^ (((row >> 3) & 1) << 1)) * 16);
      }
      const unsigned v_l = lane * 16;
      const unsigned s_ab = ((ab_e & 7) * 64 + lw * 32) * 1024;
      const unsigned s_q = lw * 32 * 3072 + q_hp * 64;
      const unsigned s_b = ((b_kq & 1) * 128 + lw * 4) * 16384 + ((b_kq >> 1) * 512 + b_nt * 128) * 16;
      const unsigned s_o = (lw * 8) * 32768 + o_pb * 1024;
      auto issue = [&](auto gc) {
        constexpr int g = gc; constexpr int ph = SC::phase(g), k = SC::idx(g);
        char* l = shm + (g % NSLOT) * SLOT;
        if constexpr (ph == 0) {
#pragma unroll
          for (int i = 0; i < 4; ++i) dma16<0>(rD, v_ab ^ (i << 5), s_ab + i * 8192 + k * 128, l + 16384 + (lw * 4 + i) * 1024);
        } else if constexpr (ph == 1) {
#pragma unroll
          for (int i = 0; i < 6; ++i) dma16<0>(rW, v_q[i % 3], s_q + (i / 3) * 16 * 3072 + k * 64 * 3072, l + 18432 + (lw * 6 + i) * 1024);
        } else if constexpr (ph == 2) {
#pragma unroll
          for (int i = 0; i < 8; ++i) dma16<0>(rAB, v_l, s_b + (k * 8 + (i >> 1)) * 16384 + (i & 1) * 1024, l + 16384 + (lw * 8 + i) * 1024);
        } else if constexpr (ph == 3) {
#pragma unroll
          for (int i = 0; i < 8; ++i) dma16<0>(rO, v_l, s_o + (k * 16 + i) * 32768, l + 16384 + (lw * 8 + i) * 1024);
        }
      };
      static_for<NSLOT - 1>([&](auto gc) { issue(gc); });
      static_for<SC::G>([&](auto gc) {
        constexpr int g = gc;
        WAIT_V(SC::cnt(1, g + 1) + SC::cnt(1, g + 2));
        BAR();
        issue(std::integral_constant<int, g + NSLOT - 1>{});
      });
    }
    return;
  }

  const int g = lane >> 4, i = lane & 15;
  if constexpr (PH & 1) {
    const int wm = wid >> 1, wn = wid & 1;
    const int f = ((i >> 2) << 1) | ((i >> 1) & 1), rin = 8 * (i >> 2) + (i & 3);
    unsigned aa[2], ba[2];
#pragma unroll
    for (int ks = 0; ks < 2; ++ks) {
      aa[ks] = (wm * 64 + rin) * 128 + (((4 * ks + g) ^ f) * 16);
      ba[ks] = 16384 + (wn * 32 + rin) * 128 + (((4 * ks + g) ^ f) * 16);
    }
    f32x4 acc[4][2] = {};
    bf16x8 fa[2][2][4], fb[2][2][2];
    auto rd = [&](int set, int slot) {
      const char* l = shm + slot * SLOT;
#pragma unroll
      for (int ks = 0; ks < 2; ++ks) {
#pragma unroll
        for (int m = 0; m < 4; ++m) fa[set][ks][m] = lds128(l + aa[ks] + (m >> 1) * 4096 + (m & 1) * 512);
#pragma unroll
        for (int n = 0; n < 2; ++n) fb[set][ks][n] = lds128(l + ba[ks] + n * 512);
      }
    };
    auto mm = [&](int set) {
#pragma unroll
      for (int ks = 0; ks < 2; ++ks)
#pragma unroll
        for (int m = 0; m < 4; ++m)
#pragma unroll
          for (int n = 0; n < 2; ++n) acc[m][n] = mma(fa[set][ks][m], fb[set][ks][n], acc[m][n]);
    };
    auto sched = [&]() {
#pragma unroll
      for (int q = 0; q < 12; ++q) { __builtin_amdgcn_sched_group_barrier(0x8, 1, 0); __builtin_amdgcn_sched_group_barrier(0x100, 1, 0); }
      __builtin_amdgcn_sched_group_barrier(0x8, 16, 0);
    };
    BAR(); rd(0, 0); WAIT_L0(); SCHED0();
#pragma unroll
    for (int k = 1; k < 8; ++k) { BAR(); rd(k & 1, k & 3); mm((k - 1) & 1); sched(); WAIT_L0(); SCHED0(); }
    mm(1);
    rsrc_t rAB = mk(ws + WS_AB, 4u << 20);
    const float sg = (ab_e >> 3) ? -1.f : 1.f;
#pragma unroll
    for (int P = 0; P < 2; ++P)
#pragma unroll
      for (int n = 0; n < 2; ++n) {
        f32x4 lo = acc[2 * P][n] * sg, hi = acc[2 * P + 1][n] * sg;
        u32x4 v = {pk(lo[0], lo[1]), pk(lo[2], lo[3]), pk(hi[0], hi[1]), pk(hi[2], hi[3])};
        unsigned c = ab_s * 16 + wm * 8 + P * 4 + g, pos = ab_e * 64 + wn * 32 + n * 16 + i;
        __builtin_amdgcn_raw_buffer_store_b128(v, rAB, (c * 1024 + pos) * 16, 0, 0);
      }
  }
  if constexpr (PH & 2) {
    const int hh = wid & 1, half = wid >> 1;
    const int q4 = i >> 2, p4 = i & 3;
    unsigned xa[2];
#pragma unroll
    for (int ks = 0; ks < 2; ++ks) xa[ks] = (half * 64 + i) * 128 + (((4 * ks + g) ^ (i >> 1)) * 16);
    const unsigned wa = 18432 + (8 * g + q4) * 192 + (((2 * hh + (p4 >> 1)) ^ ((g & 1) << 1)) * 16) + 8 * (p4 & 1);
    f32x4 aq[4] = {}, ak[5] = {}, av[5] = {};
    bf16x8 fx[2][2][5], fw[2][2][3];
    auto rd = [&](int set, int slot) {
      const char* l = shm + slot * SLOT;
#pragma unroll
      for (int ks = 0; ks < 2; ++ks) {
#pragma unroll
        for (int s = 0; s < 3; ++s) fw[set][ks][s] = cat(ldstr(l + wa + ks * 6144 + s * 64), ldstr(l + wa + ks * 6144 + 768 + s * 64));
#pragma unroll
        for (int b = 0; b < 5; ++b) fx[set][ks][b] = lds128(l + xa[ks] + b * 2048);
      }
    };
    auto mm = [&](int set) {
#pragma unroll
      for (int ks = 0; ks < 2; ++ks) {
#pragma unroll
        for (int b = 0; b < 5; ++b) {
          if (b) aq[b - 1] = mma(fw[set][ks][0], fx[set][ks][b], aq[b - 1]);
          ak[b] = mma(fw[set][ks][1], fx[set][ks][b], ak[b]);
          av[b] = mma(fx[set][ks][b], fw[set][ks][2], av[b]);
        }
      }
    };
    auto sched = [&]() {
#pragma unroll
      for (int q = 0; q < 22; ++q) { __builtin_amdgcn_sched_group_barrier(0x8, 1, 0); __builtin_amdgcn_sched_group_barrier(0x100, 1, 0); }
      __builtin_amdgcn_sched_group_barrier(0x8, 28, 0);
    };
    BAR(); rd(0, 0); WAIT_L0(); SCHED0();
#pragma unroll
    for (int k = 1; k < 8; ++k) { BAR(); rd(k & 1, k & 3); mm((k - 1) & 1); sched(); WAIT_L0(); SCHED0(); }
    mm(1);
    bf16x4 kf[5], vf[5];
#pragma unroll
    for (int b = 0; b < 5; ++b) { kf[b] = pk4(ak[b]); vf[b] = pk4(av[b]); }
    const bool first = ((q_j & 7) == 0) && half == 0;
    u32x2 ob[4];
#pragma unroll
    for (int b = 0; b < 4; ++b) {
      bf16x4 qf = pk4(aq[b]);
      f32x4 z = {};
      f32x4 sp = mma16(kf[b], qf, z), sc = mma16(kf[b + 1], qf, z);
      float l[8], mx = -3.0e38f;
#pragma unroll
      for (int r = 0; r < 4; ++r) {
        int key = 4 * g + r;
        bool vp = (key >= i + 9) && !(b == 0 && first), vc = (key <= i) && (key >= i - 7);
        l[r] = vp ? rbf(sp[r]) * 0.25f : -3.0e38f;
        l[4 + r] = vc ? rbf(sc[r]) * 0.25f : -3.0e38f;
        mx = fmaxf(mx, fmaxf(l[r], l[4 + r]));
      }
      mx = red4max(mx);
      float sum = 0.f;
#pragma unroll
      for (int r = 0; r < 8; ++r) { l[r] = __expf(l[r] - mx); sum += l[r]; }
      sum = red4sum(sum);
      float inv = 1.0f / sum;
      f32x4 pp = {l[0] * inv, l[1] * inv, l[2] * inv, l[3] * inv}, pc = {l[4] * inv, l[5] * inv, l[6] * inv, l[7] * inv};
      f32x4 o = mma16(vf[b], pk4(pp), z);
      o = mma16(vf[b + 1], pk4(pc), o);
      ob[b] = __builtin_bit_cast(u32x2, pk4(o));
    }
    rsrc_t rO = mk(ws + WS_O, 2u << 20);
#pragma unroll
    for (int P = 0; P < 2; ++P) {
      u32x4 v = swap16(ob[2 * P], ob[2 * P + 1]);
      unsigned c = (q_hp * 2 + hh) * 2 + (g >> 1), pos = q_j * 128 + half * 64 + (2 * P + (g & 1)) * 16 + i;
      __builtin_amdgcn_raw_buffer_store_b128(v, rO, (c * 2048 + pos) * 16, 0, 0);
    }
  }
  if constexpr (PH & 4) {
    const int wr = wid >> 1, wc = wid & 1;
    unsigned sb[2];
#pragma unroll
    for (int ks = 0; ks < 2; ++ks) sb[ks] = (wc * 64 + i) * 128 + (((4 * ks + g) ^ (i >> 1)) * 16);
    const unsigned ab = 16384 + g * 2048 + (wr * 64 + i) * 16;
    f32x4 acc[4][4] = {};
    bf16x8 fa[2][2][4], fb[2][2][4];
    auto rd = [&](int set, int slot) {
      const char* l = shm + slot * SLOT;
#pragma unroll
      for (int ks = 0; ks < 2; ++ks) {
#pragma unroll
        for (int m = 0; m < 4; ++m) fa[set][ks][m] = lds128(l + ab + ks * 8192 + m * 256);
#pragma unroll
        for (int n = 0; n < 4; ++n) fb[set][ks][n] = lds128(l + sb[ks] + n * 2048);
      }
    };
    auto mm = [&](int set) {
#pragma unroll
      for (int ks = 0; ks < 2; ++ks)
#pragma unroll
        for (int m = 0; m < 4; ++m)
#pragma unroll
          for (int n = 0; n < 4; ++n) acc[m][n] = mma(fa[set][ks][m], fb[set][ks][n], acc[m][n]);
    };
    auto sched = [&]() {
#pragma unroll
      for (int q = 0; q < 16; ++q) { __builtin_amdgcn_sched_group_barrier(0x8, 1, 0); __builtin_amdgcn_sched_group_barrier(0x100, 1, 0); }
      __builtin_amdgcn_sched_group_barrier(0x8, 32, 0);
    };
    BAR(); rd(0, 0); WAIT_L0(); SCHED0();
    for (int k = 1; k < 15; k += 2) {
      BAR(); rd(1, k & 3); mm(0); sched(); WAIT_L0(); SCHED0();
      BAR(); rd(0, (k + 1) & 3); mm(1); sched(); WAIT_L0(); SCHED0();
    }
    BAR(); rd(1, 3); mm(0); sched(); WAIT_L0(); SCHED0();
    mm(1);
    rsrc_t rP = mk(ws + WS_PART, 16u << 20);
    const unsigned pb = ((b_mt * 4 + b_nt) * 4 + b_kq) * 65536 + wid * 4096;
#pragma unroll
    for (int n = 0; n < 4; ++n)
#pragma unroll
      for (int m = 0; m < 4; ++m)
        __builtin_amdgcn_raw_buffer_store_b128(__builtin_bit_cast(u32x4, acc[m][n]), rP, lane * 16, pb + n * 16384 + m * 1024, 0);
  }
  if constexpr (PH & 8) {
    const int wn = wid >> 1, wp = wid & 1;
    const int q4 = i >> 2, p4 = i & 3;
    const int sw = 2 * (((g & 1) << 1) | (q4 >> 1));
    unsigned wa[2];
#pragma unroll
    for (int a = 0; a < 2; ++a) wa[a] = (8 * g + q4) * 128 + (((wn * 4 + 2 * a + (p4 >> 1)) ^ sw) * 16) + 8 * (p4 & 1);
    const unsigned oa = 16384 + g * 1024 + (wp * 32 + i) * 16;
    f32x4 acc[2][2] = {};
    bf16x8 fa[2][4][2], fb[2][4][2];
    auto rd = [&](int set, int slot) {
      const char* l = shm + slot * SLOT;
#pragma unroll
      for (int ks = 0; ks < 4; ++ks) {
#pragma unroll
        for (int a = 0; a < 2; ++a) fa[set][ks][a] = cat(ldstr(l + wa[a] + ks * 4096), ldstr(l + wa[a] + ks * 4096 + 512));
#pragma unroll
        for (int b = 0; b < 2; ++b) fb[set][ks][b] = lds128(l + oa + ks * 4096 + b * 256);
      }
    };
    auto mm = [&](int set) {
#pragma unroll
      for (int ks = 0; ks < 4; ++ks)
#pragma unroll
        for (int a = 0; a < 2; ++a)
#pragma unroll
          for (int b = 0; b < 2; ++b) acc[a][b] = mma(fa[set][ks][a], fb[set][ks][b], acc[a][b]);
    };
    BAR(); rd(0, 0); WAIT_L0(); SCHED0();
#pragma unroll
    for (int k = 1; k < 4; ++k) { BAR(); rd(k & 1, k & 3); mm((k - 1) & 1); WAIT_L0(); SCHED0(); }
    mm(1);
    rsrc_t rOut = mk(out, 4u << 20);
#pragma unroll
    for (int b = 0; b < 2; ++b) {
      u32x4 v = swap16(__builtin_bit_cast(u32x2, pk4(acc[0][b])), __builtin_bit_cast(u32x2, pk4(acc[1][b])));
      int pos = o_pb * 64 + wp * 32 + b * 16 + i, t = 2 * (pos & 1023) + (pos >> 10);
      int n = o_nt * 64 + wn * 32 + 16 * (g & 1) + 8 * (g >> 1);
      __builtin_amdgcn_raw_buffer_store_b128(v, rOut, t * 2048 + 1024 + n * 2, 0, 0);
    }
  }
  if constexpr (PH & 16) {
    const int wr = wid >> 1, wc = wid & 1;
    rsrc_t rP = mk(ws + WS_PART, 16u << 20);
    f32x4 acc[4] = {};
#pragma unroll
    for (int s = 0; s < 4; ++s)
#pragma unroll
      for (int m = 0; m < 4; ++m)
        acc[m] += __builtin_bit_cast(f32x4, __builtin_amdgcn_raw_buffer_load_b128(rP, lane * 16, ((b_mt * 4 + b_nt) * 4 + s) * 65536 + b_kq * 16384 + wid * 4096 + m * 1024, 0));
    rsrc_t rOut = mk(out, 4u << 20);
    int t = b_mt * 128 + wc * 64 + b_kq * 16 + i;
#pragma unroll
    for (int P = 0; P < 2; ++P) {
      f32x4 lo = acc[2 * P], hi = acc[2 * P + 1];
      u32x4 v = {pk(lo[0], lo[1]), pk(lo[2], lo[3]), pk(hi[0], hi[1]), pk(hi[2], hi[3])};
      int e = b_nt * 128 + wr * 64 + 32 * P + 8 * g;
      __builtin_amdgcn_raw_buffer_store_b128(v, rOut, t * 2048 + e * 2, 0, 0);
    }
  }
}

template <int PH> static void launch(void* const* d_in, void* d_out, void* d_ws, hipStream_t stream) {
  (void)hipFuncSetAttribute((const void*)fused<PH>, hipFuncAttributeMaxDynamicSharedMemorySize, NSLOT * SLOT);
  fused<PH><<<256, 512, NSLOT * SLOT, stream>>>((const char*)d_in[0], (const char*)d_in[1], (const char*)d_in[2], (const char*)d_in[3],
                                                (const char*)d_in[4], (const char*)d_in[5], (const char*)d_in[6], (const char*)d_in[7],
                                                (char*)d_out, (char*)d_ws);
}

extern "C" void kernel_launch(void* const* d_in, const int* in_sizes, int n_in, void* d_out, int out_size, void* d_ws, size_t ws_size,
                              hipStream_t stream) {
  launch<3>(d_in, d_out, d_ws, stream);
  launch<12>(d_in, d_out, d_ws, stream);
  launch<16>(d_in, d_out, d_ws, stream);
}
```

```cpp
#include <hip/hip_runtime.h>
#include <hip/hip_bf16.h>
#include <utility>

typedef __attribute__((ext_vector_type(8))) short bf16x8;
typedef __attribute__((ext_vector_type(4))) short bf16x4;
typedef __attribute__((ext_vector_type(4))) float f32x4;
typedef __attribute__((ext_vector_type(2))) float f32x2;
typedef __attribute__((ext_vector_type(2))) __bf16 bf2;
typedef __attribute__((ext_vector_type(4))) unsigned u32x4;
typedef __attribute__((ext_vector_type(2))) unsigned u32x2;
typedef unsigned long long u64;
typedef __amdgpu_buffer_rsrc_t rsrc_t;
#define LAS __attribute__((address_space(3)))
#define GAS __attribute__((address_space(1)))
#define DEV __device__ __forceinline__

#define WAIT_V(n) asm volatile("s_waitcnt vmcnt(%0)" ::"n"(n) : "memory")
#define WAIT_L0() asm volatile("s_waitcnt lgkmcnt(0)" ::: "memory")
#define BAR() do{asm volatile("" ::: "memory"); __builtin_amdgcn_s_barrier(); asm volatile("" ::: "memory");}while(0)
#define SCHED0() __builtin_amdgcn_sched_barrier(0)
#define SGB(m, n) __builtin_amdgcn_sched_group_barrier(m, n, 0)

constexpr int SLOT = 32768, LDS_BYTES = 163840;
constexpr int QSLOT = 43008, QBASE = LDS_BYTES - 3 * QSLOT, QW = 18432;
constexpr unsigned WS_AB = 0, WS_O = 4u << 20, WS_FLAG = 6u << 20, WS_PART = 8u << 20;
constexpr unsigned FL_AB = 128, FL_O = 1024, FL_P = 2048;
constexpr int SC1 = 16;

#define AUX_S 0
#define ROT1 1
#define ROT2 1

#define STAMP(n)
#define LSTAMP(n)

template <int AUX> DEV void dma16(rsrc_t r, unsigned voff, unsigned soff, char* lds) {
  __builtin_amdgcn_raw_ptr_buffer_load_lds(r, (LAS void*)lds, 16, voff, soff, 0, AUX);
}
DEV rsrc_t mk(const void* p, unsigned bytes) { return __builtin_amdgcn_make_buffer_rsrc((void*)p, 0, bytes, 0x00020000); }
DEV unsigned pk(float a, float b) { f32x2 v = {a, b}; return __builtin_bit_cast(unsigned, __builtin_convertvector(v, bf2)); }
DEV u32x2 pk4u(f32x4 v) { u32x2 r = {pk(v[0], v[1]), pk(v[2], v[3])}; return r; }
DEV bf16x4 pk4(f32x4 v) { return __builtin_bit_cast(bf16x4, pk4u(v)); }
DEV u32x4 pk8(f32x4 lo, f32x4 hi) { u32x4 v = {pk(lo[0], lo[1]), pk(lo[2], lo[3]), pk(hi[0], hi[1]), pk(hi[2], hi[3])}; return v; }
DEV float blo(unsigned u) { return __builtin_bit_cast(float, u << 16); }
DEV float bhi(unsigned u) { return __builtin_bit_cast(float, u & 0xffff0000u); }
DEV bf16x8 lds128(const char* p) { return *(const bf16x8*)p; }
DEV bf16x4 ldstr(const char* p) { return __builtin_amdgcn_ds_read_tr16_b64_v4i16((LAS bf16x4*)p); }
DEV bf16x8 cat(bf16x4 a, bf16x4 b) { return __builtin_shufflevector(a, b, 0, 1, 2, 3, 4, 5, 6, 7); }
DEV f32x4 mma(bf16x8 a, bf16x8 b, f32x4 c) { return __builtin_amdgcn_mfma_f32_16x16x32_bf16(a, b, c, 0, 0, 0); }
DEV f32x4 mma16(bf16x4 a, bf16x4 b, f32x4 c) { return __builtin_amdgcn_mfma_f32_16x16x16bf16_1k(a, b, c, 0, 0, 0); }
DEV float vmax(float a, float b) { float r; asm("v_max_f32 %0, %1, %2" : "=v"(r) : "v"(a), "v"(b)); return r; }
DEV float vmax3(float a, float b, float c) { float r; asm("v_max3_f32 %0, %1, %2, %3" : "=v"(r) : "v"(a), "v"(b), "v"(c)); return r; }
DEV unsigned cpy(unsigned u) { asm("" : "+v"(u)); return u; }
DEV float red4max(float x) {
  unsigned u = __builtin_bit_cast(unsigned, x);
  auto a = __builtin_amdgcn_permlane32_swap(u, cpy(u), false, false);
  unsigned a0 = a[0], a1 = a[1];
  x = vmax(__builtin_bit_cast(float, a0), __builtin_bit_cast(float, a1));
  u = __builtin_bit_cast(unsigned, x);
  auto b = __builtin_amdgcn_permlane16_swap(u, cpy(u), false, false);
  unsigned b0 = b[0], b1 = b[1];
  return vmax(__builtin_bit_cast(float, b0), __builtin_bit_cast(float, b1));
}
DEV u32x4 swap16(u32x2 x, u32x2 y) {
  auto a = __builtin_amdgcn_permlane16_swap(x[0], y[0], false, false);
  auto b = __builtin_amdgcn_permlane16_swap(x[1], y[1], false, false);
  unsigned a0 = a[0], a1 = a[1], b0 = b[0], b1 = b[1];
  u32x4 r = {a0, b0, a1, b1};
  return r;
}
DEV u64 ld_agent(const u64* p) { return __hip_atomic_load((GAS const u64*)p, __ATOMIC_RELAXED, __HIP_MEMORY_SCOPE_AGENT); }
DEV void st_agent(u64* p, u64 v) { __hip_atomic_store((GAS u64*)p, v, __ATOMIC_RELAXED, __HIP_MEMORY_SCOPE_AGENT); }
DEV u64 ld_async(const u64* p) { u64 v; asm volatile("global_load_dwordx2 %0, %1, off sc1" : "=v"(v) : "v"(p) : "memory"); return v; }
DEV void landed(u64& v) { asm volatile("" : "+v"(v)::"memory"); }
DEV u64 mk_tag(u64 e) {
  unsigned lo = __builtin_amdgcn_readfirstlane((unsigned)e), hi = __builtin_amdgcn_readfirstlane((unsigned)(e >> 32));
  u64 x = ((u64)hi << 32) | lo;
  x = (x ^ (x >> 31)) * 0x9E3779B97F4A7C15ull + 0xD1B54A32D192ED03ull;
  return x ^ (x >> 29);
}
DEV u64 get_tag(const u64* fl) { return mk_tag(ld_agent(fl)); }
DEV void wait_flags(const u64* p, u64 tag) {
  for (int spins = 0; spins < (1 << 16); ++spins) {
    u64 v = ld_agent(p);
    if (__all(v == tag)) break;
    __builtin_amdgcn_s_sleep(2);
  }
  asm volatile("" ::: "memory");
}
DEV void publish(u64* p, u64 tag, int lane) {
  WAIT_V(0);
  if (lane == 0) st_agent(p, tag);
}

__global__ __launch_bounds__(512, 2) void fused(const char* __restrict__ xA, const char* __restrict__ Sr, const char* __restrict__ Si,
                                                const char* __restrict__ Dr, const char* __restrict__ Di, const char* __restrict__ xB,
                                                const char* __restrict__ wqkv, const char* __restrict__ wo, char* __restrict__ out,
                                                char* __restrict__ ws) {
  extern __shared__ __attribute__((aligned(16))) char shm[];
  const int bid = blockIdx.x, xcd = bid & 7, loc = bid >> 3;
  const int wid = __builtin_amdgcn_readfirstlane(threadIdx.x >> 6), lane = threadIdx.x & 63;
  u64* const fl = (u64*)(ws + WS_FLAG);
  const bool isq = loc >= 16;
  const int l16 = loc & 15;
  const int p_r = 4 * (xcd & 3) + (l16 & 3), p_c = 4 * (xcd >> 2) + (l16 >> 2);
  const int b_kq = xcd & 3, b_mt = 8 * (xcd >> 2) + (loc >> 2), b_nt = loc & 3;
  const int o_pb = 8 * (xcd & 3) + (loc & 7), o_nt = 4 * (xcd >> 2) + (loc >> 3);

  if (wid >= 4) {
    __builtin_amdgcn_s_setprio(3);
    const int lw = wid & 1;
    const int r = lane >> 3, p = lane & 7;
    const int k1 = ROT1 ? 2 * ((l16 & 3) + (l16 >> 2)) : 0;
    const int k2 = ROT2 ? 4 * b_nt + 2 * (loc >> 2) : 0;
    if (wid < 6) {
      rsrc_t rS = mk((b_kq >> 1) ? Si : Sr, 8u << 20), rWo = mk(wo, 512u << 10);
      const unsigned v_s = r * 4096 + ((p ^ (r >> 1)) * 16);
      const unsigned v_w = r * 1024 + ((p ^ (((r >> 1) & 1) * 2)) * 16);
      const unsigned s_s = (b_mt * 128 + lw * 64) * 4096 + (b_kq & 1) * 2048;
      const unsigned s_w = (lw * 64) * 1024 + o_nt * 128;
      auto issueS = [&](int k, int slot) {
        char* l = shm + slot * SLOT + lw * 8192;
        unsigned so = s_s + ((k + k2) & 15) * 128;
#pragma unroll
        for (int i = 0; i < 8; ++i) dma16<AUX_S>(rS, v_s ^ ((i & 1) << 6), so + i * 32768, l + i * 1024);
      };
      auto issueW = [&](int k, int slot) {
        char* l = shm + slot * SLOT + lw * 8192;
#pragma unroll
        for (int i = 0; i < 8; ++i) dma16<0>(rWo, v_w ^ ((i & 1) << 6), s_w + i * 8192 + k * 131072, l + i * 1024);
      };
      if (!isq) {
        rsrc_t rX = mk(xA, 2u << 20);
        const unsigned v = r * 1024 + ((p ^ ((r >> 1) & 1)) * 16);
        const unsigned s0 = (p_r * 128 + lw * 64) * 1024;
        auto issue = [&](int k) {
          char* l = shm + ((k + 2) % 5) * SLOT + lw * 8192;
          unsigned so = s0 + ((k + k1) & 7) * 128;
#pragma unroll
          for (int i = 0; i < 8; ++i) dma16<0>(rX, v ^ ((i & 3) << 5), so + i * 8192, l + i * 1024);
        };
#pragma unroll
        for (int k = 0; k < 4; ++k) issue(k);
#pragma unroll
        for (int g = 0; g < 9; ++g) { WAIT_V(24); BAR(); if (g < 4) issue(g + 4); else issueS(g - 4, g - 4); }
      } else {
        rsrc_t rX = mk(xB, 2u << 20);
        const unsigned v = r * 2048 + ((p ^ (r >> 1)) * 16);
        const int row0 = ((p_r & 7) * 128 - 16) * 2 + (p_r >> 3);
        auto issue = [&](int k) {
          char* l = shm + QBASE + ((k + 1) % 3) * QSLOT;
          unsigned ko = ((k + k1) & 7) * 128;
#pragma unroll
          for (int i = 0; i < 9; ++i) {
            int ii = lw * 9 + i;
            int row = row0 + ii * 16; if (row < 0) row += 32;
            dma16<0>(rX, v ^ ((ii & 1) << 6), row * 1024 + ko, l + ii * 1024);
          }
        };
        issue(0); issue(1); issue(2);
        WAIT_V(18); BAR();
#pragma unroll
        for (int g = 1; g < 6; ++g) { WAIT_V(9); BAR(); issue(g + 2); }
        issueS(0, 0);
        WAIT_V(17); BAR(); issueS(1, 1);
        WAIT_V(16); BAR(); issueS(2, 2);
        WAIT_V(16); BAR(); issueS(3, 3); issueS(4, 4);
      }
      int slot = 0;
#pragma unroll 1
      for (int k = 5; k < 16; ++k) { WAIT_V(24); BAR(); issueS(k, slot); slot = slot == 4 ? 0 : slot + 1; }
#pragma unroll
      for (int k = 0; k < 4; ++k) { WAIT_V(24); BAR(); issueW(k, (16 + k) % 5); }
      WAIT_V(24); BAR(); WAIT_V(16); BAR(); WAIT_V(8); BAR(); WAIT_V(0); BAR();
      if (bid == 0 && wid == 4) {
        u64 tag = get_tag(fl);
#pragma unroll 1
        for (int spins = 0; spins < (1 << 16); ++spins) {
          bool ok = true;
#pragma unroll
          for (int j = 0; j < 8; ++j) ok &= (ld_agent(fl + FL_AB + j * 64 + lane) == tag) & (ld_agent(fl + FL_O + j * 64 + lane) == tag);
          if (__all(ok)) break;
          __builtin_amdgcn_s_sleep(2);
        }
        if (lane == 0) st_agent(fl, tag);
      }
    } else {
      u64 ep = ld_async(fl), tag;
      rsrc_t rAB = mk(ws + WS_AB, 4u << 20), rO = mk(ws + WS_O, 2u << 20);
      const unsigned v_l = lane * 16;
      const unsigned s_b = ((b_kq & 1) * 128 + lw * 4) * 16384 + ((b_kq >> 1) * 512 + b_nt * 128) * 16;
      const unsigned s_o = (lw * 8) * 32768 + o_pb * 1024;
      auto issueA = [&](int k, int slot) {
        char* l = shm + slot * SLOT + 16384 + lw * 8192;
        unsigned so = s_b + ((k + k2) & 15) * 131072;
#pragma unroll
        for (int i = 0; i < 8; ++i) dma16<SC1>(rAB, v_l, so + (i >> 1) * 16384 + (i & 1) * 1024, l + i * 1024);
      };
      auto issueO = [&](int k, int slot) {
        char* l = shm + slot * SLOT + 16384 + lw * 8192;
#pragma unroll
        for (int i = 0; i < 8; ++i) dma16<SC1>(rO, v_l, s_o + (k * 16 + i) * 32768, l + i * 1024);
      };
      const u64* fab = fl + FL_AB + (((b_kq >> 1) * 4 + b_nt) * 16 + (b_kq & 1) * 8) * 4 + (lane & 31);
      if (!isq) {
        rsrc_t rD = mk((p_c >> 2) ? Di : Dr, 512u << 10);
        const unsigned v = r * 1024 + ((p ^ ((r >> 1) & 1)) * 16);
        const unsigned s0 = ((p_c & 3) * 128 + lw * 64) * 1024;
        auto issue = [&](int k) {
          char* l = shm + ((k + 2) % 5) * SLOT + 16384 + lw * 8192;
          unsigned so = s0 + ((k + k1) & 7) * 128;
#pragma unroll
          for (int i = 0; i < 8; ++i) dma16<0>(rD, v ^ ((i & 3) << 5), so + i * 8192, l + i * 1024);
        };
#pragma unroll
        for (int k = 0; k < 4; ++k) issue(k);
#pragma unroll
        for (int g = 0; g < 4; ++g) { WAIT_V(24); BAR(); issue(g + 4); }
        WAIT_V(24); BAR(); WAIT_V(16); BAR(); WAIT_V(8); BAR(); WAIT_V(0); BAR();
        LSTAMP(16);
        landed(ep); tag = mk_tag(ep);
        wait_flags(fab, tag);
        LSTAMP(17);
        issueA(0, 0); issueA(1, 1); issueA(2, 2); issueA(3, 3);
        WAIT_V(24); BAR(); issueA(4, 4);
      } else {
        rsrc_t rW = mk(wqkv, 1536u << 10);
        unsigned v[3];
#pragma unroll
        for (int i = 0; i < 3; ++i) {
          int idx = 64 * i + lane, row = idx / 24, cc = idx % 24;
          v[i] = row * 3072 + (cc >> 3) * 1024 + (((cc & 7) ^ (((row >> 1) & 1) * 2)) * 16);
        }
        const unsigned s0 = lw * 32 * 3072 + p_c * 128;
        auto issue = [&](int k) {
          char* l = shm + QBASE + ((k + 1) % 3) * QSLOT + QW + lw * 12288;
          unsigned so = s0 + ((k + k1) & 7) * 64 * 3072;
#pragma unroll
          for (int i = 0; i < 12; ++i) dma16<0>(rW, v[i % 3] ^ (((i / 3) & 1) << 6), so + (i / 3) * 8 * 3072, l + i * 1024);
        };
        issue(0); issue(1); issue(2);
        WAIT_V(24); BAR();
#pragma unroll
        for (int g = 1; g < 6; ++g) { WAIT_V(12); BAR(); issue(g + 2); }
        WAIT_V(12); BAR(); WAIT_V(0); BAR();
        LSTAMP(16);
        landed(ep); tag = mk_tag(ep);
        wait_flags(fab, tag);
        LSTAMP(17);
        issueA(0, 0); issueA(1, 1); issueA(2, 2);
        WAIT_V(16); BAR(); issueA(3, 3); issueA(4, 4);
      }
      int slot = 0;
#pragma unroll 1
      for (int k = 5; k < 12; ++k) { WAIT_V(24); BAR(); issueA(k, slot); slot = slot == 4 ? 0 : slot + 1; }
      const u64* fo = fl + FL_O + (o_pb >> 1) * 32 + (lane & 31);
      u64 of = ld_async(fo);
#pragma unroll 1
      for (int k = 12; k < 16; ++k) { WAIT_V(24); BAR(); issueA(k, slot); slot = slot == 4 ? 0 : slot + 1; }
      WAIT_V(24); BAR();
      landed(of);
      if (!__all(of == tag)) wait_flags(fo, tag);
      issueO(0, 1);
      WAIT_V(24); BAR(); issueO(1, 2);
      WAIT_V(24); BAR(); issueO(2, 3);
      WAIT_V(24); BAR(); issueO(3, 4);
      WAIT_V(24); BAR(); WAIT_V(16); BAR(); WAIT_V(8); BAR(); WAIT_V(0); BAR();
    }
    return;
  }

  const int g = lane >> 4, i = lane & 15;
  const int wr = wid >> 1, wc = wid & 1;
  STAMP(0);
  const u64 tag = get_tag(fl);
  if (!isq) {
    const int f = ((i >> 2) << 1) | ((i >> 1) & 1), rin = 8 * (i >> 2) + (i & 3);
    unsigned aa[2], ba[2];
#pragma unroll
    for (int ks = 0; ks < 2; ++ks) {
      aa[ks] = (wr * 64 + rin) * 128 + (((4 * ks + g) ^ f) * 16);
      ba[ks] = 16384 + (wc * 64 + rin) * 128 + (((4 * ks + g) ^ f) * 16);
    }
    f32x4 acc[4][4] = {};
    bf16x8 fa[2][4], fb[2][4];
    auto rd = [&](int ks, int slot) {
      const char* l = shm + slot * SLOT;
#pragma unroll
      for (int m = 0; m < 4; ++m) fa[ks][m] = lds128(l + aa[ks] + (m >> 1) * 4096 + (m & 1) * 512);
#pragma unroll
      for (int n = 0; n < 4; ++n) fb[ks][n] = lds128(l + ba[ks] + (n >> 1) * 4096 + (n & 1) * 512);
    };
    auto mm = [&](int ks) {
#pragma unroll
      for (int m = 0; m < 4; ++m)
#pragma unroll
        for (int n = 0; n < 4; ++n) acc[m][n] = mma(fa[ks][m], fb[ks][n], acc[m][n]);
    };
    auto sched = [&]() {
#pragma unroll
      for (int q = 0; q < 8; ++q) { SGB(0x8, 1); SGB(0x100, 1); }
      SGB(0x8, 8);
    };
    int slot = 2;
    BAR(); rd(0, slot); SCHED0(); rd(1, slot); mm(0); sched(); WAIT_L0(); SCHED0();
#pragma unroll 1
    for (int k = 1; k < 8; ++k) {
      slot = slot == 4 ? 0 : slot + 1;
      BAR(); rd(0, slot); mm(1); sched(); SCHED0(); rd(1, slot); mm(0); sched(); WAIT_L0(); SCHED0();
    }
    mm(1);
    STAMP(1);
    rsrc_t rAB = mk(ws + WS_AB, 4u << 20);
    const float sg = (p_c >> 2) ? -1.f : 1.f;
    const unsigned vo = ((p_r * 16 + wr * 8 + g) * 1024 + p_c * 128 + wc * 64 + i) * 16;
#pragma unroll
    for (int P = 0; P < 2; ++P)
#pragma unroll
      for (int n = 0; n < 4; ++n)
        __builtin_amdgcn_raw_buffer_store_b128(pk8(acc[2 * P][n] * sg, acc[2 * P + 1][n] * sg), rAB, vo + P * 65536 + n * 256, 0, SC1);
    STAMP(2);
    publish(fl + FL_AB + (p_c * 16 + p_r) * 4 + wid, tag, lane);
    STAMP(3);
  } else {
    const int q4 = i >> 2, p4 = i & 3;
    unsigned xa[2];
#pragma unroll
    for (int ks = 0; ks < 2; ++ks) xa[ks] = QBASE + i * 128 + (((4 * ks + g) ^ (i >> 1)) * 16);
    const unsigned wa = QBASE + QW + (8 * g + q4) * 384 + (((2 * wid + (p4 >> 1)) ^ (2 * (((g & 1) << 1) | (q4 >> 1)))) * 16) + 8 * (p4 & 1);
    f32x4 aq[8] = {}, ak[9] = {}, av[9] = {};
    bf16x8 fx[2][9], fw[2][3];
    auto rd = [&](int ks, int slot) {
      const char* l = shm + slot * QSLOT;
#pragma unroll
      for (int s = 0; s < 3; ++s) fw[ks][s] = cat(ldstr(l + wa + ks * 12288 + s * 128), ldstr(l + wa + ks * 12288 + 1536 + s * 128));
#pragma unroll
      for (int b = 0; b < 9; ++b) fx[ks][b] = lds128(l + xa[ks] + b * 2048);
    };
    auto mm = [&](int ks) {
#pragma unroll
      for (int b = 0; b < 9; ++b) {
        if (b) aq[b - 1] = mma(fw[ks][0], fx[ks][b], aq[b - 1]);
        ak[b] = mma(fw[ks][1], fx[ks][b], ak[b]);
        av[b] = mma(fx[ks][b], fw[ks][2], av[b]);
      }
    };
    auto sched = [&]() {
#pragma unroll
      for (int q = 0; q < 15; ++q) { SGB(0x8, 1); SGB(0x100, 1); }
      SGB(0x8, 11);
    };
    int slot = 1;
    BAR(); rd(0, slot); SCHED0(); rd(1, slot); mm(0); sched(); WAIT_L0(); SCHED0();
#pragma unroll 1
    for (int k = 1; k < 8; ++k) {
      slot = slot == 2 ? 0 : slot + 1;
      BAR(); rd(0, slot); mm(1); sched(); SCHED0(); rd(1, slot); mm(0); sched(); WAIT_L0(); SCHED0();
    }
    mm(1);
    STAMP(1);
    bf16x4 kf[9], vf[9];
#pragma unroll
    for (int b = 0; b < 9; ++b) { kf[b] = pk4(ak[b]); vf[b] = pk4(av[b]); }
    constexpr float NEG = -3.0e38f;
    f32x4 cp, cc, c0;
    u32x2 mp, mc;
#pragma unroll
    for (int r = 0; r < 4; ++r) {
      int key = 4 * g + r;
      bool vp = key >= i + 9, vc = (key <= i) && (key >= i - 7);
      cp[r] = vp ? 0.f : NEG; cc[r] = vc ? 0.f : NEG;
      c0[r] = ((p_r & 7) == 0) ? NEG : cp[r];
      if (!(r & 1)) { mp[r >> 1] = vp ? 0xffffu : 0u; mc[r >> 1] = vc ? 0xffffu : 0u; }
      else { mp[r >> 1] |= vp ? 0xffff0000u : 0u; mc[r >> 1] |= vc ? 0xffff0000u : 0u; }
    }
    const bf16x4 ones = {0x3f80, 0x3f80, 0x3f80, 0x3f80};
    constexpr float CE = 0.25f * 1.4426950408889634f;
    u32x2 ob[8];
#pragma unroll
    for (int b = 0; b < 8; ++b) {
      bf16x4 qf = pk4(aq[b]);
      f32x4 sp = mma16(kf[b], qf, b == 0 ? c0 : cp), sc = mma16(kf[b + 1], qf, cc);
      f32x4 s = {fmaxf(sp[0], sc[0]), fmaxf(sp[1], sc[1]), fmaxf(sp[2], sc[2]), fmaxf(sp[3], sc[3])};
      u32x2 sb = pk4u(s);
      float l0 = blo(sb[0]), l1 = bhi(sb[0]), l2 = blo(sb[1]), l3 = bhi(sb[1]);
      float mx = red4max(vmax(vmax3(l0, l1, l2), l3));
      float mc2 = mx * CE;
      f32x4 e = {__builtin_amdgcn_exp2f(l0 * CE - mc2), __builtin_amdgcn_exp2f(l1 * CE - mc2), __builtin_amdgcn_exp2f(l2 * CE - mc2),
                 __builtin_amdgcn_exp2f(l3 * CE - mc2)};
      u32x2 eb = pk4u(e);
      f32x4 z = {};
      f32x4 sum = mma16(ones, __builtin_bit_cast(bf16x4, eb), z);
      f32x4 o = mma16(vf[b], __builtin_bit_cast(bf16x4, eb & mp), z);
      o = mma16(vf[b + 1], __builtin_bit_cast(bf16x4, eb & mc), o);
      float inv = __builtin_amdgcn_rcpf(sum[0]);
      ob[b] = pk4u(o * inv);
    }
    rsrc_t rO = mk(ws + WS_O, 2u << 20);
    const unsigned vo = (((p_c * 4 + wid) * 2 + (g >> 1)) * 2048 + p_r * 128 + (g & 1) * 16 + i) * 16;
#pragma unroll
    for (int P = 0; P < 4; ++P) __builtin_amdgcn_raw_buffer_store_b128(swap16(ob[2 * P], ob[2 * P + 1]), rO, vo + P * 512, 0, SC1);
    STAMP(2);
    publish(fl + FL_O + (p_r * 8 + p_c) * 4 + wid, tag, lane);
    STAMP(3);
  }
  f32x4 keep[4];
  {
    unsigned sb[2];
#pragma unroll
    for (int ks = 0; ks < 2; ++ks) sb[ks] = (wc * 64 + i) * 128 + (((4 * ks + g) ^ (i >> 1)) * 16);
    const unsigned ab = 16384 + g * 2048 + (wr * 64 + i) * 16;
    f32x4 acc[4][4] = {};
    bf16x8 fa[2][4], fb[2][4];
    auto rd = [&](int ks, int slot) {
      const char* l = shm + slot * SLOT;
#pragma unroll
      for (int m = 0; m < 4; ++m) fa[ks][m] = lds128(l + ab + ks * 8192 + m * 256);
#pragma unroll
      for (int n = 0; n < 4; ++n) fb[ks][n] = lds128(l + sb[ks] + n * 2048);
    };
    auto mm = [&](int ks) {
#pragma unroll
      for (int m = 0; m < 4; ++m)
#pragma unroll
        for (int n = 0; n < 4; ++n) acc[m][n] = mma(fa[ks][m], fb[ks][n], acc[m][n]);
    };
    auto sched = [&]() {
#pragma unroll
      for (int q = 0; q < 8; ++q) { SGB(0x8, 1); SGB(0x100, 1); }
      SGB(0x8, 8);
    };
    int slot = 0;
    BAR();
    STAMP(4);
    rd(0, slot); SCHED0(); rd(1, slot); mm(0); sched(); WAIT_L0(); SCHED0();
#pragma unroll 1
    for (int k = 1; k < 16; ++k) {
      slot = slot == 4 ? 0 : slot + 1;
      BAR(); rd(0, slot); mm(1); sched(); SCHED0(); rd(1, slot); mm(0); sched(); WAIT_L0(); SCHED0();
    }
    mm(1);
    STAMP(5);
    rsrc_t rP = mk(ws + WS_PART, 8u << 20);
    const unsigned pb = (b_mt * 4 + b_nt) * 131072 + b_kq * 8192 + wid * 2048;
#pragma unroll
    for (int n = 0; n < 4; ++n) {
      if (n != b_kq) {
#pragma unroll
        for (int P = 0; P < 2; ++P)
          __builtin_amdgcn_raw_buffer_store_b128(pk8(acc[2 * P][n], acc[2 * P + 1][n]), rP, pb + n * 32768 + lane * 16 + P * 1024, 0, SC1);
      } else {
#pragma unroll
        for (int m = 0; m < 4; ++m) keep[m] = acc[m][n];
      }
    }
    STAMP(6);
  }
  {
    const int q4 = i >> 2, p4 = i & 3;
    const int sw = 2 * (((g & 1) << 1) | (q4 >> 1));
    unsigned wa[2];
#pragma unroll
    for (int a = 0; a < 2; ++a) wa[a] = (8 * g + q4) * 128 + (((wr * 4 + 2 * a + (p4 >> 1)) ^ sw) * 16) + 8 * (p4 & 1);
    const unsigned oa = 16384 + g * 1024 + (wc * 32 + i) * 16;
    f32x4 acc[2][2] = {};
    bf16x8 fa[4][2], fb[4][2];
    auto rd = [&](int slot) {
      const char* l = shm + slot * SLOT;
#pragma unroll
      for (int ks = 0; ks < 4; ++ks) {
#pragma unroll
        for (int a = 0; a < 2; ++a) fa[ks][a] = cat(ldstr(l + wa[a] + ks * 4096), ldstr(l + wa[a] + ks * 4096 + 512));
#pragma unroll
        for (int b = 0; b < 2; ++b) fb[ks][b] = lds128(l + oa + ks * 4096 + b * 256);
      }
    };
    auto mm = [&]() {
#pragma unroll
      for (int ks = 0; ks < 4; ++ks)
#pragma unroll
        for (int a = 0; a < 2; ++a)
#pragma unroll
          for (int b = 0; b < 2; ++b) acc[a][b] = mma(fa[ks][a], fb[ks][b], acc[a][b]);
    };
    BAR(); rd(1); mm(); SCHED0();
    publish(fl + FL_P + ((b_mt * 4 + b_nt) * 4 + wid) * 4 + b_kq, tag, lane);
    STAMP(7);
#pragma unroll 1
    for (int k = 1; k < 4; ++k) { BAR(); rd(1 + k); mm(); SCHED0(); }
    STAMP(8);
    rsrc_t rOut = mk(out, 4u << 20);
#pragma unroll
    for (int b = 0; b < 2; ++b) {
      u32x4 v = swap16(pk4u(acc[0][b]), pk4u(acc[1][b]));
      int pos = o_pb * 64 + wc * 32 + b * 16 + i, t = 2 * (pos & 1023) + (pos >> 10);
      int n = o_nt * 64 + wr * 32 + 16 * (g & 1) + 8 * (g >> 1);
      __builtin_amdgcn_raw_buffer_store_b128(v, rOut, t * 2048 + 1024 + n * 2, 0, 0);
    }
  }
  {
    wait_flags(fl + FL_P + ((b_mt * 4 + b_nt) * 4 + wid) * 4 + (lane & 3), tag);
    STAMP(9);
    rsrc_t rP = mk(ws + WS_PART, 8u << 20);
    const unsigned pb = (b_mt * 4 + b_nt) * 131072 + b_kq * 32768 + wid * 2048;
    u32x4 v[3][2];
#pragma unroll
    for (int s = 0; s < 3; ++s)
#pragma unroll
      for (int P = 0; P < 2; ++P)
        v[s][P] = __builtin_amdgcn_raw_buffer_load_b128(rP, lane * 16 + P * 1024, pb + ((b_kq + 1 + s) & 3) * 8192, SC1);
    rsrc_t rOut = mk(out, 4u << 20);
    int t = b_mt * 128 + wc * 64 + b_kq * 16 + i;
#pragma unroll
    for (int P = 0; P < 2; ++P) {
      f32x4 lo = keep[2 * P], hi = keep[2 * P + 1];
#pragma unroll
      for (int s = 0; s < 3; ++s) {
        f32x4 a = {blo(v[s][P][0]), bhi(v[s][P][0]), blo(v[s][P][1]), bhi(v[s][P][1])};
        f32x4 b = {blo(v[s][P][2]), bhi(v[s][P][2]), blo(v[s][P][3]), bhi(v[s][P][3])};
        lo += a; hi += b;
      }
      int e = b_nt * 128 + wr * 64 + 32 * P + 8 * g;
      __builtin_amdgcn_raw_buffer_store_b128(pk8(lo, hi), rOut, t * 2048 + e * 2, 0, 0);
    }
    STAMP(10);
  }
}

extern "C" void kernel_launch(void* const* d_in, const int* in_sizes, int n_in, void* d_out, int out_size, void* d_ws, size_t ws_size,
                              hipStream_t stream) {
  (void)hipFuncSetAttribute((const void*)fused, hipFuncAttributeMaxDynamicSharedMemorySize, LDS_BYTES);
  fused<<<256, 512, LDS_BYTES, stream>>>((const char*)d_in[0], (const char*)d_in[1], (const char*)d_in[2], (const char*)d_in[3],
                                         (const char*)d_in[4], (const char*)d_in[5], (const char*)d_in[6], (const char*)d_in[7],
                                         (char*)d_out, (char*)d_ws);
}
```

```cpp
#define PRETOUCH 0
#include <hip/hip_runtime.h>
#include <hip/hip_bf16.h>
#include <utility>

typedef __attribute__((ext_vector_type(8))) short bf16x8;
typedef __attribute__((ext_vector_type(4))) short bf16x4;
typedef __attribute__((ext_vector_type(4))) float f32x4;
typedef __attribute__((ext_vector_type(2))) float f32x2;
typedef __attribute__((ext_vector_type(2))) __bf16 bf2;
typedef __attribute__((ext_vector_type(4))) unsigned u32x4;
typedef __attribute__((ext_vector_type(2))) unsigned u32x2;
typedef unsigned long long u64;
typedef __amdgpu_buffer_rsrc_t rsrc_t;
#define LAS __attribute__((address_space(3)))
#define GAS __attribute__((address_space(1)))
#define DEV __device__ __forceinline__

#define WAIT_V(n) asm volatile("s_waitcnt vmcnt(%0)" ::"n"(n) : "memory")
#define WAIT_L0() asm volatile("s_waitcnt lgkmcnt(0)" ::: "memory")
#define BAR() do{asm volatile("" ::: "memory"); __builtin_amdgcn_s_barrier(); asm volatile("" ::: "memory");}while(0)
#define SCHED0() __builtin_amdgcn_sched_barrier(0)
#define SGB(m, n) __builtin_amdgcn_sched_group_barrier(m, n, 0)

constexpr int SLOT = 32768, LDS_BYTES = 163840;
constexpr int QSLOT = 43008, QBASE = LDS_BYTES - 3 * QSLOT, QW = 18432;
constexpr unsigned WS_AB = 0, WS_O = 4u << 20, WS_FLAG = 6u << 20, WS_PART = 8u << 20;
constexpr unsigned FL_AB = 128, FL_O = 1024, FL_P = 2048;
constexpr int SC1 = 16;

#define AUX_S 0
#define ROT1 1
#define ROT2 1

#define STAMP(n)
#define LSTAMP(n)

template <int AUX> DEV void dma16(rsrc_t r, unsigned voff, unsigned soff, char* lds) {
  __builtin_amdgcn_raw_ptr_buffer_load_lds(r, (LAS void*)lds, 16, voff, soff, 0, AUX);
}
DEV rsrc_t mk(const void* p, unsigned bytes) { return __builtin_amdgcn_make_buffer_rsrc((void*)p, 0, bytes, 0x00020000); }
DEV unsigned pk(float a, float b) { f32x2 v = {a, b}; return __builtin_bit_cast(unsigned, __builtin_convertvector(v, bf2)); }
DEV u32x2 pk4u(f32x4 v) { u32x2 r = {pk(v[0], v[1]), pk(v[2], v[3])}; return r; }
DEV bf16x4 pk4(f32x4 v) { return __builtin_bit_cast(bf16x4, pk4u(v)); }
DEV u32x4 pk8(f32x4 lo, f32x4 hi) { u32x4 v = {pk(lo[0], lo[1]), pk(lo[2], lo[3]), pk(hi[0], hi[1]), pk(hi[2], hi[3])}; return v; }
DEV float blo(unsigned u) { return __builtin_bit_cast(float, u << 16); }
DEV float bhi(unsigned u) { return __builtin_bit_cast(float, u & 0xffff0000u); }
DEV bf16x8 lds128(const char* p) { return *(const bf16x8*)p; }
DEV bf16x4 ldstr(const char* p) { return __builtin_amdgcn_ds_read_tr16_b64_v4i16((LAS bf16x4*)p); }
DEV bf16x8 cat(bf16x4 a, bf16x4 b) { return __builtin_shufflevector(a, b, 0, 1, 2, 3, 4, 5, 6, 7); }
DEV f32x4 mma(bf16x8 a, bf16x8 b, f32x4 c) { return __builtin_amdgcn_mfma_f32_16x16x32_bf16(a, b, c, 0, 0, 0); }
DEV f32x4 mma16(bf16x4 a, bf16x4 b, f32x4 c) { return __builtin_amdgcn_mfma_f32_16x16x16bf16_1k(a, b, c, 0, 0, 0); }
DEV float vmax(float a, float b) { float r; asm("v_max_f32 %0, %1, %2" : "=v"(r) : "v"(a), "v"(b)); return r; }
DEV float vmax3(float a, float b, float c) { float r; asm("v_max3_f32 %0, %1, %2, %3" : "=v"(r) : "v"(a), "v"(b), "v"(c)); return r; }
DEV unsigned cpy(unsigned u) { asm("" : "+v"(u)); return u; }
DEV float red4max(float x) {
  unsigned u = __builtin_bit_cast(unsigned, x);
  auto a = __builtin_amdgcn_permlane32_swap(u, cpy(u), false, false);
  unsigned a0 = a[0], a1 = a[1];
  x = vmax(__builtin_bit_cast(float, a0), __builtin_bit_cast(float, a1));
  u = __builtin_bit_cast(unsigned, x);
  auto b = __builtin_amdgcn_permlane16_swap(u, cpy(u), false, false);
  unsigned b0 = b[0], b1 = b[1];
  return vmax(__builtin_bit_cast(float, b0), __builtin_bit_cast(float, b1));
}
DEV u32x4 swap16(u32x2 x, u32x2 y) {
  auto a = __builtin_amdgcn_permlane16_swap(x[0], y[0], false, false);
  auto b = __builtin_amdgcn_permlane16_swap(x[1], y[1], false, false);
  unsigned a0 = a[0], a1 = a[1], b0 = b[0], b1 = b[1];
  u32x4 r = {a0, b0, a1, b1};
  return r;
}
DEV u64 ld_agent(const u64* p) { return __hip_atomic_load((GAS const u64*)p, __ATOMIC_RELAXED, __HIP_MEMORY_SCOPE_AGENT); }
DEV void st_agent(u64* p, u64 v) { __hip_atomic_store((GAS u64*)p, v, __ATOMIC_RELAXED, __HIP_MEMORY_SCOPE_AGENT); }
DEV u64 ld_async(const u64* p) { u64 v; asm volatile("global_load_dwordx2 %0, %1, off sc1" : "=v"(v) : "v"(p) : "memory"); return v; }
DEV void landed(u64& v) { asm volatile("" : "+v"(v)::"memory"); }
DEV u64 mk_tag(u64 e) {
  unsigned lo = __builtin_amdgcn_readfirstlane((unsigned)e), hi = __builtin_amdgcn_readfirstlane((unsigned)(e >> 32));
  u64 x = ((u64)hi << 32) | lo;
  x = (x ^ (x >> 31)) * 0x9E3779B97F4A7C15ull + 0xD1B54A32D192ED03ull;
  return x ^ (x >> 29);
}
DEV u64 get_tag(const u64* fl) { return mk_tag(ld_agent(fl)); }
DEV void wait_flags(const u64* p, u64 tag) {
  for (int spins = 0; spins < (1 << 16); ++spins) {
    u64 v = ld_agent(p);
    if (__all(v == tag)) break;
    __builtin_amdgcn_s_sleep(2);
  }
  asm volatile("" ::: "memory");
}
DEV void publish(u64* p, u64 tag, int lane) {
  WAIT_V(0);
  if (lane == 0) st_agent(p, tag);
}

__global__ __launch_bounds__(512, 2) void fused(const char* __restrict__ xA, const char* __restrict__ Sr, const char* __restrict__ Si,
                                                const char* __restrict__ Dr, const char* __restrict__ Di, const char* __restrict__ xB,
                                                const char* __restrict__ wqkv, const char* __restrict__ wo, char* __restrict__ out,
                                                char* __restrict__ ws) {
  extern __shared__ __attribute__((aligned(16))) char shm[];
  const int bid = blockIdx.x, xcd = bid & 7, loc = bid >> 3;
  const int wid = __builtin_amdgcn_readfirstlane(threadIdx.x >> 6), lane = threadIdx.x & 63;
  u64* const fl = (u64*)(ws + WS_FLAG);
  const bool isq = loc >= 16;
  const int l16 = loc & 15;
  const int p_r = 4 * (xcd & 3) + (l16 & 3), p_c = 4 * (xcd >> 2) + (l16 >> 2);
  const int b_kq = xcd & 3, b_mt = 8 * (xcd >> 2) + (loc >> 2), b_nt = loc & 3;
  const int o_pb = 8 * (xcd & 3) + (loc & 7), o_nt = 4 * (xcd >> 2) + (loc >> 3);

  if (wid >= 4) {
    __builtin_amdgcn_s_setprio(3);
    const int lw = wid & 1;
    const int r = lane >> 3, p = lane & 7;
    const int k1 = ROT1 ? 2 * ((l16 & 3) + (l16 >> 2)) : 0;
    const int k2 = ROT2 ? 4 * b_nt + 2 * (loc >> 2) : 0;
    if (wid < 6) {
      rsrc_t rS = mk((b_kq >> 1) ? Si : Sr, 8u << 20), rWo = mk(wo, 512u << 10);
      const unsigned v_s = r * 4096 + ((p ^ (r >> 1)) * 16);
      const unsigned v_w = r * 1024 + ((p ^ (((r >> 1) & 1) * 2)) * 16);
      const unsigned s_s = (b_mt * 128 + lw * 64) * 4096 + (b_kq & 1) * 2048;
      const unsigned s_w = (lw * 64) * 1024 + o_nt * 128;
      auto issueS = [&](int k, int slot) {
        char* l = shm + slot * SLOT + lw * 8192;
        unsigned so = s_s + ((k + k2) & 15) * 128;
#pragma unroll
        for (int i = 0; i < 8; ++i) dma16<AUX_S>(rS, v_s ^ ((i & 1) << 6), so + i * 32768, l + i * 1024);
      };
      auto issueW = [&](int k, int slot) {
        char* l = shm + slot * SLOT + lw * 8192;
#pragma unroll
        for (int i = 0; i < 8; ++i) dma16<0>(rWo, v_w ^ ((i & 1) << 6), s_w + i * 8192 + k * 131072, l + i * 1024);
      };
      if (!isq) {
        rsrc_t rX = mk(xA, 2u << 20);
        const unsigned v = r * 1024 + ((p ^ ((r >> 1) & 1)) * 16);
        const unsigned s0 = (p_r * 128 + lw * 64) * 1024;
        auto issue = [&](int k) {
          char* l = shm + ((k + 2) % 5) * SLOT + lw * 8192;
          unsigned so = s0 + ((k + k1) & 7) * 128;
#pragma unroll
          for (int i = 0; i < 8; ++i) dma16<0>(rX, v ^ ((i & 3) << 5), so + i * 8192, l + i * 1024);
        };
#pragma unroll
        for (int k = 0; k < 4; ++k) issue(k);
#pragma unroll
        for (int g = 0; g < 9; ++g) { WAIT_V(24); BAR(); if (g < 4) issue(g + 4); else issueS(g - 4, g - 4); }
      } else {
        rsrc_t rX = mk(xB, 2u << 20);
        const unsigned v = r * 2048 + ((p ^ (r >> 1)) * 16);
        const int row0 = ((p_r & 7) * 128 - 16) * 2 + (p_r >> 3);
        auto issue = [&](int k) {
          char* l = shm + QBASE + ((k + 1) % 3) * QSLOT;
          unsigned ko = ((k + k1) & 7) * 128;
#pragma unroll
          for (int i = 0; i < 9; ++i) {
            int ii = lw * 9 + i;
            int row = row0 + ii * 16; if (row < 0) row += 32;
            dma16<0>(rX, v ^ ((ii & 1) << 6), row * 1024 + ko, l + ii * 1024);
          }
        };
        issue(0); issue(1); issue(2);
        WAIT_V(18); BAR();
#pragma unroll
        for (int g = 1; g < 6; ++g) { WAIT_V(9); BAR(); issue(g + 2); }
        issueS(0, 0);
        WAIT_V(17); BAR(); issueS(1, 1);
        WAIT_V(16); BAR(); issueS(2, 2);
        WAIT_V(16); BAR(); issueS(3, 3); issueS(4, 4);
      }
      int slot = 0;
#pragma unroll 1
      for (int k = 5; k < 16; ++k) { WAIT_V(24); BAR(); issueS(k, slot); slot = slot == 4 ? 0 : slot + 1; }
#pragma unroll
      for (int k = 0; k < 4; ++k) { WAIT_V(24); BAR(); issueW(k, (16 + k) % 5); }
      WAIT_V(24); BAR(); WAIT_V(16); BAR(); WAIT_V(8); BAR(); WAIT_V(0); BAR();
      if (bid == 0 && wid == 4) {
        u64 tag = get_tag(fl);
#pragma unroll 1
        for (int spins = 0; spins < (1 << 16); ++spins) {
          bool ok = true;
#pragma unroll
          for (int j = 0; j < 8; ++j) ok &= (ld_agent(fl + FL_AB + j * 64 + lane) == tag) & (ld_agent(fl + FL_O + j * 64 + lane) == tag);
          if (__all(ok)) break;
          __builtin_amdgcn_s_sleep(2);
        }
        if (lane == 0) st_agent(fl, tag);
      }
    } else {
      u64 ep = ld_async(fl), tag;
      rsrc_t rAB = mk(ws + WS_AB, 4u << 20), rO = mk(ws + WS_O, 2u << 20);
      const unsigned v_l = lane * 16;
      const unsigned s_b = ((b_kq & 1) * 128 + lw * 4) * 16384 + ((b_kq >> 1) * 512 + b_nt * 128) * 16;
      const unsigned s_o = (lw * 8) * 32768 + o_pb * 1024;
      auto issueA = [&](int k, int slot) {
        char* l = shm + slot * SLOT + 16384 + lw * 8192;
        unsigned so = s_b + ((k + k2) & 15) * 131072;
#pragma unroll
        for (int i = 0; i < 8; ++i) dma16<SC1>(rAB, v_l, so + (i >> 1) * 16384 + (i & 1) * 1024, l + i * 1024);
      };
      auto issueO = [&](int k, int slot) {
        char* l = shm + slot * SLOT + 16384 + lw * 8192;
#pragma unroll
        for (int i = 0; i < 8; ++i) dma16<SC1>(rO, v_l, s_o + (k * 16 + i) * 32768, l + i * 1024);
      };
      const u64* fab = fl + FL_AB + (((b_kq >> 1) * 4 + b_nt) * 16 + (b_kq & 1) * 8) * 4 + (lane & 31);
      if (!isq) {
        rsrc_t rD = mk((p_c >> 2) ? Di : Dr, 512u << 10);
        const unsigned v = r * 1024 + ((p ^ ((r >> 1) & 1)) * 16);
        const unsigned s0 = ((p_c & 3) * 128 + lw * 64) * 1024;
        auto issue = [&](int k) {
          char* l = shm + ((k + 2) % 5) * SLOT + 16384 + lw * 8192;
          unsigned so = s0 + ((k + k1) & 7) * 128;
#pragma unroll
          for (int i = 0; i < 8; ++i) dma16<0>(rD, v ^ ((i & 3) << 5), so + i * 8192, l + i * 1024);
        };
#pragma unroll
        for (int k = 0; k < 4; ++k) issue(k);
#pragma unroll
        for (int g = 0; g < 4; ++g) { WAIT_V(24); BAR(); issue(g + 4); }
        WAIT_V(24); BAR(); WAIT_V(16); BAR(); WAIT_V(8); BAR(); WAIT_V(0); BAR();
        LSTAMP(16);
        landed(ep); tag = mk_tag(ep);
        wait_flags(fab, tag);
        LSTAMP(17);
        issueA(0, 0); issueA(1, 1); issueA(2, 2); issueA(3, 3);
        WAIT_V(24); BAR(); issueA(4, 4);
      } else {
        rsrc_t rW = mk(wqkv, 1536u << 10);
        unsigned v[3];
#pragma unroll
        for (int i = 0; i < 3; ++i) {
          int idx = 64 * i + lane, row = idx / 24, cc = idx % 24;
          v[i] = row * 3072 + (cc >> 3) * 1024 + (((cc & 7) ^ (((row >> 1) & 1) * 2)) * 16);
        }
        const unsigned s0 = lw * 32 * 3072 + p_c * 128;
        auto issue = [&](int k) {
          char* l = shm + QBASE + ((k + 1) % 3) * QSLOT + QW + lw * 12288;
          unsigned so = s0 + ((k + k1) & 7) * 64 * 3072;
#pragma unroll
          for (int i = 0; i < 12; ++i) dma16<0>(rW, v[i % 3] ^ (((i / 3) & 1) << 6), so + (i / 3) * 8 * 3072, l + i * 1024);
        };
        issue(0); issue(1); issue(2);
        WAIT_V(24); BAR();
#pragma unroll
        for (int g = 1; g < 6; ++g) { WAIT_V(12); BAR(); issue(g + 2); }
        WAIT_V(12); BAR(); WAIT_V(0); BAR();
        LSTAMP(16);
        landed(ep); tag = mk_tag(ep);
        wait_flags(fab, tag);
        LSTAMP(17);
        issueA(0, 0); issueA(1, 1); issueA(2, 2);
        WAIT_V(16); BAR(); issueA(3, 3); issueA(4, 4);
      }
      int slot = 0;
#pragma unroll 1
      for (int k = 5; k < 12; ++k) { WAIT_V(24); BAR(); issueA(k, slot); slot = slot == 4 ? 0 : slot + 1; }
      const u64* fo = fl + FL_O + (o_pb >> 1) * 32 + (lane & 31);
      u64 of = ld_async(fo);
#pragma unroll 1
      for (int k = 12; k < 16; ++k) { WAIT_V(24); BAR(); issueA(k, slot); slot = slot == 4 ? 0 : slot + 1; }
      WAIT_V(24); BAR();
      landed(of);
      if (!__all(of == tag)) wait_flags(fo, tag);
      issueO(0, 1);
      WAIT_V(24); BAR(); issueO(1, 2);
      WAIT_V(24); BAR(); issueO(2, 3);
      WAIT_V(24); BAR(); issueO(3, 4);
      WAIT_V(24); BAR(); WAIT_V(16); BAR(); WAIT_V(8); BAR(); WAIT_V(0); BAR();
    }
    return;
  }

  const int g = lane >> 4, i = lane & 15;
  const int wr = wid >> 1, wc = wid & 1;
  STAMP(0);
  {
    __builtin_amdgcn_s_sleep(PRETOUCH);
    const char *p0, *p1, *p2;
    if (wid == 0) {
      const char* S = (b_kq >> 1) ? Si : Sr;
      p0 = S + (b_mt * 128 + lane) * 4096 + (b_kq & 1) * 2048; p1 = p0 + 64 * 4096;
      p2 = ws + WS_PART + (b_mt * 4 + b_nt) * 131072 + (lane & 31) * 4096;
    } else if (wid == 1) {
      p0 = ws + WS_AB + ((b_kq & 1) * 128 + lane) * 16384 + ((b_kq >> 1) * 512 + b_nt * 128) * 16; p1 = p0 + 64 * 16384;
      p2 = isq ? ws + WS_O + (p_c * 8 + (lane & 7)) * 32768 + p_r * 2048 : ws + WS_AB + (p_r * 16 + (lane & 15)) * 16384 + p_c * 2048;
    } else if (wid == 2) {
      p0 = wo + lane * 4096 + o_nt * 128; p1 = p0 + 64 * 4096;
      p2 = ws + WS_O + lane * 32768 + o_pb * 1024;
    } else {
      int pos = o_pb * 64 + lane, t = 2 * (pos & 1023) + (pos >> 10);
      p0 = out + t * 2048 + 1024 + o_nt * 128;
      p1 = out + (b_mt * 128 + (lane >> 5) * 64 + b_kq * 16 + (lane & 15)) * 2048 + b_nt * 256;
      p2 = p1;
    }
    unsigned d0, d1, d2;
    asm volatile("global_load_dword %0, %3, off sc1\n\tglobal_load_dword %1, %4, off sc1\n\tglobal_load_dword %2, %5, off sc1\n\ts_waitcnt vmcnt(0)"
                 : "=&v"(d0), "=&v"(d1), "=&v"(d2) : "v"(p0), "v"(p1), "v"(p2) : "memory");
  }
  const u64 tag = get_tag(fl);
  if (!isq) {
    const int f = ((i >> 2) << 1) | ((i >> 1) & 1), rin = 8 * (i >> 2) + (i & 3);
    unsigned aa[2], ba[2];
#pragma unroll
    for (int ks = 0; ks < 2; ++ks) {
      aa[ks] = (wr * 64 + rin) * 128 + (((4 * ks + g) ^ f) * 16);
      ba[ks] = 16384 + (wc * 64 + rin) * 128 + (((4 * ks + g) ^ f) * 16);
    }
    f32x4 acc[4][4] = {};
    bf16x8 fa[2][4], fb[2][4];
    auto rd = [&](int ks, int slot) {
      const char* l = shm + slot * SLOT;
#pragma unroll
      for (int m = 0; m < 4; ++m) fa[ks][m] = lds128(l + aa[ks] + (m >> 1) * 4096 + (m & 1) * 512);
#pragma unroll
      for (int n = 0; n < 4; ++n) fb[ks][n] = lds128(l + ba[ks] + (n >> 1) * 4096 + (n & 1) * 512);
    };
    auto mm = [&](int ks) {
#pragma unroll
      for (int m = 0; m < 4; ++m)
#pragma unroll
        for (int n = 0; n < 4; ++n) acc[m][n] = mma(fa[ks][m], fb[ks][n], acc[m][n]);
    };
    auto sched = [&]() {
#pragma unroll
      for (int q = 0; q < 8; ++q) { SGB(0x8, 1); SGB(0x100, 1); }
      SGB(0x8, 8);
    };
    int slot = 2;
    BAR(); rd(0, slot); SCHED0(); rd(1, slot); mm(0); sched(); WAIT_L0(); SCHED0();
#pragma unroll 1
    for (int k = 1; k < 8; ++k) {
      slot = slot == 4 ? 0 : slot + 1;
      BAR(); rd(0, slot); mm(1); sched(); SCHED0(); rd(1, slot); mm(0); sched(); WAIT_L0(); SCHED0();
    }
    mm(1);
    STAMP(1);
    rsrc_t rAB = mk(ws + WS_AB, 4u << 20);
    const float sg = (p_c >> 2) ? -1.f : 1.f;
    const unsigned vo = ((p_r * 16 + wr * 8 + g) * 1024 + p_c * 128 + wc * 64 + i) * 16;
#pragma unroll
    for (int P = 0; P < 2; ++P)
#pragma unroll
      for (int n = 0; n < 4; ++n)
        __builtin_amdgcn_raw_buffer_store_b128(pk8(acc[2 * P][n] * sg, acc[2 * P + 1][n] * sg), rAB, vo + P * 65536 + n * 256, 0, SC1);
    STAMP(2);
    publish(fl + FL_AB + (p_c * 16 + p_r) * 4 + wid, tag, lane);
    STAMP(3);
  } else {
    const int q4 = i >> 2, p4 = i & 3;
    unsigned xa[2];
#pragma unroll
    for (int ks = 0; ks < 2; ++ks) xa[ks] = QBASE + i * 128 + (((4 * ks + g) ^ (i >> 1)) * 16);
    const unsigned wa = QBASE + QW + (8 * g + q4) * 384 + (((2 * wid + (p4 >> 1)) ^ (2 * (((g & 1) << 1) | (q4 >> 1)))) * 16) + 8 * (p4 & 1);
    f32x4 aq[8] = {}, ak[9] = {}, av[9] = {};
    bf16x8 fx[2][9], fw[2][3];
    auto rd = [&](int ks, int slot) {
      const char* l = shm + slot * QSLOT;
#pragma unroll
      for (int s = 0; s < 3; ++s) fw[ks][s] = cat(ldstr(l + wa + ks * 12288 + s * 128), ldstr(l + wa + ks * 12288 + 1536 + s * 128));
#pragma unroll
      for (int b = 0; b < 9; ++b) fx[ks][b] = lds128(l + xa[ks] + b * 2048);
    };
    auto mm = [&](int ks) {
#pragma unroll
      for (int b = 0; b < 9; ++b) {
        if (b) aq[b - 1] = mma(fw[ks][0], fx[ks][b], aq[b - 1]);
        ak[b] = mma(fw[ks][1], fx[ks][b], ak[b]);
        av[b] = mma(fx[ks][b], fw[ks][2], av[b]);
      }
    };
    auto sched = [&]() {
#pragma unroll
      for (int q = 0; q < 15; ++q) { SGB(0x8, 1); SGB(0x100, 1); }
      SGB(0x8, 11);
    };
    int slot = 1;
    BAR(); rd(0, slot); SCHED0(); rd(1, slot); mm(0); sched(); WAIT_L0(); SCHED0();
#pragma unroll 1
    for (int k = 1; k < 8; ++k) {
      slot = slot == 2 ? 0 : slot + 1;
      BAR(); rd(0, slot); mm(1); sched(); SCHED0(); rd(1, slot); mm(0); sched(); WAIT_L0(); SCHED0();
    }
    mm(1);
    STAMP(1);
    bf16x4 kf[9], vf[9];
#pragma unroll
    for (int b = 0; b < 9; ++b) { kf[b] = pk4(ak[b]); vf[b] = pk4(av[b]); }
    constexpr float NEG = -3.0e38f;
    f32x4 cp, cc, c0;
    u32x2 mp, mc;
#pragma unroll
    for (int r = 0; r < 4; ++r) {
      int key = 4 * g + r;
      bool vp = key >= i + 9, vc = (key <= i) && (key >= i - 7);
      cp[r] = vp ? 0.f : NEG; cc[r] = vc ? 0.f : NEG;
      c0[r] = ((p_r & 7) == 0) ? NEG : cp[r];
      if (!(r & 1)) { mp[r >> 1] = vp ? 0xffffu : 0u; mc[r >> 1] = vc ? 0xffffu : 0u; }
      else { mp[r >> 1] |= vp ? 0xffff0000u : 0u; mc[r >> 1] |= vc ? 0xffff0000u : 0u; }
    }
    const bf16x4 ones = {0x3f80, 0x3f80, 0x3f80, 0x3f80};
    constexpr float CE = 0.25f * 1.4426950408889634f;
    u32x2 ob[8];
#pragma unroll
    for (int b = 0; b < 8; ++b) {
      bf16x4 qf = pk4(aq[b]);
      f32x4 sp = mma16(kf[b], qf, b == 0 ? c0 : cp), sc = mma16(kf[b + 1], qf, cc);
      f32x4 s = {fmaxf(sp[0], sc[0]), fmaxf(sp[1], sc[1]), fmaxf(sp[2], sc[2]), fmaxf(sp[3], sc[3])};
      u32x2 sb = pk4u(s);
      float l0 = blo(sb[0]), l1 = bhi(sb[0]), l2 = blo(sb[1]), l3 = bhi(sb[1]);
      float mx = red4max(vmax(vmax3(l0, l1, l2), l3));
      float mc2 = mx * CE;
      f32x4 e = {__builtin_amdgcn_exp2f(l0 * CE - mc2), __builtin_amdgcn_exp2f(l1 * CE - mc2), __builtin_amdgcn_exp2f(l2 * CE - mc2),
                 __builtin_amdgcn_exp2f(l3 * CE - mc2)};
      u32x2 eb = pk4u(e);
      f32x4 z = {};
      f32x4 sum = mma16(ones, __builtin_bit_cast(bf16x4, eb), z);
      f32x4 o = mma16(vf[b], __builtin_bit_cast(bf16x4, eb & mp), z);
      o = mma16(vf[b + 1], __builtin_bit_cast(bf16x4, eb & mc), o);
      float inv = __builtin_amdgcn_rcpf(sum[0]);
      ob[b] = pk4u(o * inv);
    }
    rsrc_t rO = mk(ws + WS_O, 2u << 20);
    const unsigned vo = (((p_c * 4 + wid) * 2 + (g >> 1)) * 2048 + p_r * 128 + (g & 1) * 16 + i) * 16;
#pragma unroll
    for (int P = 0; P < 4; ++P) __builtin_amdgcn_raw_buffer_store_b128(swap16(ob[2 * P], ob[2 * P + 1]), rO, vo + P * 512, 0, SC1);
    STAMP(2);
    publish(fl + FL_O + (p_r * 8 + p_c) * 4 + wid, tag, lane);
    STAMP(3);
  }
  f32x4 keep[4];
  {
    unsigned sb[2];
#pragma unroll
    for (int ks = 0; ks < 2; ++ks) sb[ks] = (wc * 64 + i) * 128 + (((4 * ks + g) ^ (i >> 1)) * 16);
    const unsigned ab = 16384 + g * 2048 + (wr * 64 + i) * 16;
    f32x4 acc[4][4] = {};
    bf16x8 fa[2][4], fb[2][4];
    auto rd = [&](int ks, int slot) {
      const char* l = shm + slot * SLOT;
#pragma unroll
      for (int m = 0; m < 4; ++m) fa[ks][m] = lds128(l + ab + ks * 8192 + m * 256);
#pragma unroll
      for (int n = 0; n < 4; ++n) fb[ks][n] = lds128(l + sb[ks] + n * 2048);
    };
    auto mm = [&](int ks) {
#pragma unroll
      for (int m = 0; m < 4; ++m)
#pragma unroll
        for (int n = 0; n < 4; ++n) acc[m][n] = mma(fa[ks][m], fb[ks][n], acc[m][n]);
    };
    auto sched = [&]() {
#pragma unroll
      for (int q = 0; q < 8; ++q) { SGB(0x8, 1); SGB(0x100, 1); }
      SGB(0x8, 8);
    };
    int slot = 0;
    BAR();
    STAMP(4);
    rd(0, slot); SCHED0(); rd(1, slot); mm(0); sched(); WAIT_L0(); SCHED0();
#pragma unroll 1
    for (int k = 1; k < 16; ++k) {
      slot = slot == 4 ? 0 : slot + 1;
      BAR(); rd(0, slot); mm(1); sched(); SCHED0(); rd(1, slot); mm(0); sched(); WAIT_L0(); SCHED0();
    }
    mm(1);
    STAMP(5);
    rsrc_t rP = mk(ws + WS_PART, 8u << 20);
    const unsigned pb = (b_mt * 4 + b_nt) * 131072 + b_kq * 8192 + wid * 2048;
#pragma unroll
    for (int n = 0; n < 4; ++n) {
      if (n != b_kq) {
#pragma unroll
        for (int P = 0; P < 2; ++P)
          __builtin_amdgcn_raw_buffer_store_b128(pk8(acc[2 * P][n], acc[2 * P + 1][n]), rP, pb + n * 32768 + lane * 16 + P * 1024, 0, SC1);
      } else {
#pragma unroll
        for (int m = 0; m < 4; ++m) keep[m] = acc[m][n];
      }
    }
    STAMP(6);
  }
  {
    const int q4 = i >> 2, p4 = i & 3;
    const int sw = 2 * (((g & 1) << 1) | (q4 >> 1));
    unsigned wa[2];
#pragma unroll
    for (int a = 0; a < 2; ++a) wa[a] = (8 * g + q4) * 128 + (((wr * 4 + 2 * a + (p4 >> 1)) ^ sw) * 16) + 8 * (p4 & 1);
    const unsigned oa = 16384 + g * 1024 + (wc * 32 + i) * 16;
    f32x4 acc[2][2] = {};
    bf16x8 fa[4][2], fb[4][2];
    auto rd = [&](int slot) {
      const char* l = shm + slot * SLOT;
#pragma unroll
      for (int ks = 0; ks < 4; ++ks) {
#pragma unroll
        for (int a = 0; a < 2; ++a) fa[ks][a] = cat(ldstr(l + wa[a] + ks * 4096), ldstr(l + wa[a] + ks * 4096 + 512));
#pragma unroll
        for (int b = 0; b < 2; ++b) fb[ks][b] = lds128(l + oa + ks * 4096 + b * 256);
      }
    };
    auto mm = [&]() {
#pragma unroll
      for (int ks = 0; ks < 4; ++ks)
#pragma unroll
        for (int a = 0; a < 2; ++a)
#pragma unroll
          for (int b = 0; b < 2; ++b) acc[a][b] = mma(fa[ks][a], fb[ks][b], acc[a][b]);
    };
    BAR(); rd(1); mm(); SCHED0();
    publish(fl + FL_P + ((b_mt * 4 + b_nt) * 4 + wid) * 4 + b_kq, tag, lane);
    STAMP(7);
#pragma unroll 1
    for (int k = 1; k < 4; ++k) { BAR(); rd(1 + k); mm(); SCHED0(); }
    STAMP(8);
    rsrc_t rOut = mk(out, 4u << 20);
#pragma unroll
    for (int b = 0; b < 2; ++b) {
      u32x4 v = swap16(pk4u(acc[0][b]), pk4u(acc[1][b]));
      int pos = o_pb * 64 + wc * 32 + b * 16 + i, t = 2 * (pos & 1023) + (pos >> 10);
      int n = o_nt * 64 + wr * 32 + 16 * (g & 1) + 8 * (g >> 1);
      __builtin_amdgcn_raw_buffer_store_b128(v, rOut, t * 2048 + 1024 + n * 2, 0, 0);
    }
  }
  {
    wait_flags(fl + FL_P + ((b_mt * 4 + b_nt) * 4 + wid) * 4 + (lane & 3), tag);
    STAMP(9);
    rsrc_t rP = mk(ws + WS_PART, 8u << 20);
    const unsigned pb = (b_mt * 4 + b_nt) * 131072 + b_kq * 32768 + wid * 2048;
    u32x4 v[3][2];
#pragma unroll
    for (int s = 0; s < 3; ++s)
#pragma unroll
      for (int P = 0; P < 2; ++P)
        v[s][P] = __builtin_amdgcn_raw_buffer_load_b128(rP, lane * 16 + P * 1024, pb + ((b_kq + 1 + s) & 3) * 8192, SC1);
    rsrc_t rOut = mk(out, 4u << 20);
    int t = b_mt * 128 + wc * 64 + b_kq * 16 + i;
#pragma unroll
    for (int P = 0; P < 2; ++P) {
      f32x4 lo = keep[2 * P], hi = keep[2 * P + 1];
#pragma unroll
      for (int s = 0; s < 3; ++s) {
        f32x4 a = {blo(v[s][P][0]), bhi(v[s][P][0]), blo(v[s][P][1]), bhi(v[s][P][1])};
        f32x4 b = {blo(v[s][P][2]), bhi(v[s][P][2]), blo(v[s][P][3]), bhi(v[s][P][3])};
        lo += a; hi += b;
      }
      int e = b_nt * 128 + wr * 64 + 32 * P + 8 * g;
      __builtin_amdgcn_raw_buffer_store_b128(pk8(lo, hi), rOut, t * 2048 + e * 2, 0, 0);
    }
    STAMP(10);
  }
}

extern "C" void kernel_launch(void* const* d_in, const int* in_sizes, int n_in, void* d_out, int out_size, void* d_ws, size_t ws_size,
                              hipStream_t stream) {
  (void)hipFuncSetAttribute((const void*)fused, hipFuncAttributeMaxDynamicSharedMemorySize, LDS_BYTES);
  fused<<<256, 512, LDS_BYTES, stream>>>((const char*)d_in[0], (const char*)d_in[1], (const char*)d_in[2], (const char*)d_in[3],
                                         (const char*)d_in[4], (const char*)d_in[5], (const char*)d_in[6], (const char*)d_in[7],
                                         (char*)d_out, (char*)d_ws);
}
```

```cpp
#define AMP 1
#define AMPB 0
#include <hip/hip_runtime.h>
#include <hip/hip_bf16.h>
#include <utility>

typedef __attribute__((ext_vector_type(8))) short bf16x8;
typedef __attribute__((ext_vector_type(4))) short bf16x4;
typedef __attribute__((ext_vector_type(4))) float f32x4;
typedef __attribute__((ext_vector_type(2))) float f32x2;
typedef __attribute__((ext_vector_type(2))) __bf16 bf2;
typedef __attribute__((ext_vector_type(4))) unsigned u32x4;
typedef __attribute__((ext_vector_type(2))) unsigned u32x2;
typedef unsigned long long u64;
typedef __amdgpu_buffer_rsrc_t rsrc_t;
#define LAS __attribute__((address_space(3)))
#define GAS __attribute__((address_space(1)))
#define DEV __device__ __forceinline__

#define WAIT_V(n) asm volatile("s_waitcnt vmcnt(%0)" ::"n"(n) : "memory")
#define WAIT_L0() asm volatile("s_waitcnt lgkmcnt(0)" ::: "memory")
#define BAR() do{asm volatile("" ::: "memory"); __builtin_amdgcn_s_barrier(); asm volatile("" ::: "memory");}while(0)
#define SCHED0() __builtin_amdgcn_sched_barrier(0)
#define SGB(m, n) __builtin_amdgcn_sched_group_barrier(m, n, 0)

constexpr int SLOT = 32768, LDS_BYTES = 163840;
constexpr int QSLOT = 43008, QBASE = LDS_BYTES - 3 * QSLOT, QW = 18432;
constexpr unsigned WS_AB = 0, WS_O = 4u << 20, WS_FLAG = 6u << 20, WS_PART = 8u << 20;
constexpr unsigned FL_AB = 128, FL_O = 1024, FL_P = 2048;
constexpr int SC1 = 16;

#define AUX_S 0
#define ROT1 1
#define ROT2 1

#define STAMP(n) do{ if((n)==0) amp0=wall_clock64(); if((n)==AMP) amp1=wall_clock64(); if((n)==10 && blockIdx.x==AMPB && wid==0){ u64 tn=wall_clock64(); while(wall_clock64()-tn < 10*(amp1-amp0)) __builtin_amdgcn_s_sleep(1);} }while(0)
#define LSTAMP(n)

template <int AUX> DEV void dma16(rsrc_t r, unsigned voff, unsigned soff, char* lds) {
  __builtin_amdgcn_raw_ptr_buffer_load_lds(r, (LAS void*)lds, 16, voff, soff, 0, AUX);
}
DEV rsrc_t mk(const void* p, unsigned bytes) { return __builtin_amdgcn_make_buffer_rsrc((void*)p, 0, bytes, 0x00020000); }
DEV unsigned pk(float a, float b) { f32x2 v = {a, b}; return __builtin_bit_cast(unsigned, __builtin_convertvector(v, bf2)); }
DEV u32x2 pk4u(f32x4 v) { u32x2 r = {pk(v[0], v[1]), pk(v[2], v[3])}; return r; }
DEV bf16x4 pk4(f32x4 v) { return __builtin_bit_cast(bf16x4, pk4u(v)); }
DEV u32x4 pk8(f32x4 lo, f32x4 hi) { u32x4 v = {pk(lo[0], lo[1]), pk(lo[2], lo[3]), pk(hi[0], hi[1]), pk(hi[2], hi[3])}; return v; }
DEV float blo(unsigned u) { return __builtin_bit_cast(float, u << 16); }
DEV float bhi(unsigned u) { return __builtin_bit_cast(float, u & 0xffff0000u); }
DEV bf16x8 lds128(const char* p) { return *(const bf16x8*)p; }
DEV bf16x4 ldstr(const char* p) { return __builtin_amdgcn_ds_read_tr16_b64_v4i16((LAS bf16x4*)p); }
DEV bf16x8 cat(bf16x4 a, bf16x4 b) { return __builtin_shufflevector(a, b, 0, 1, 2, 3, 4, 5, 6, 7); }
DEV f32x4 mma(bf16x8 a, bf16x8 b, f32x4 c) { return __builtin_amdgcn_mfma_f32_16x16x32_bf16(a, b, c, 0, 0, 0); }
DEV f32x4 mma16(bf16x4 a, bf16x4 b, f32x4 c) { return __builtin_amdgcn_mfma_f32_16x16x16bf16_1k(a, b, c, 0, 0, 0); }
DEV float vmax(float a, float b) { float r; asm("v_max_f32 %0, %1, %2" : "=v"(r) : "v"(a), "v"(b)); return r; }
DEV float vmax3(float a, float b, float c) { float r; asm("v_max3_f32 %0, %1, %2, %3" : "=v"(r) : "v"(a), "v"(b), "v"(c)); return r; }
DEV unsigned cpy(unsigned u) { asm("" : "+v"(u)); return u; }
DEV float red4max(float x) {
  unsigned u = __builtin_bit_cast(unsigned, x);
  auto a = __builtin_amdgcn_permlane32_swap(u, cpy(u), false, false);
  unsigned a0 = a[0], a1 = a[1];
  x = vmax(__builtin_bit_cast(float, a0), __builtin_bit_cast(float, a1));
  u = __builtin_bit_cast(unsigned, x);
  auto b = __builtin_amdgcn_permlane16_swap(u, cpy(u), false, false);
  unsigned b0 = b[0], b1 = b[1];
  return vmax(__builtin_bit_cast(float, b0), __builtin_bit_cast(float, b1));
}
DEV u32x4 swap16(u32x2 x, u32x2 y) {
  auto a = __builtin_amdgcn_permlane16_swap(x[0], y[0], false, false);
  auto b = __builtin_amdgcn_permlane16_swap(x[1], y[1], false, false);
  unsigned a0 = a[0], a1 = a[1], b0 = b[0], b1 = b[1];
  u32x4 r = {a0, b0, a1, b1};
  return r;
}
DEV u64 ld_agent(const u64* p) { return __hip_atomic_load((GAS const u64*)p, __ATOMIC_RELAXED, __HIP_MEMORY_SCOPE_AGENT); }
DEV void st_agent(u64* p, u64 v) { __hip_atomic_store((GAS u64*)p, v, __ATOMIC_RELAXED, __HIP_MEMORY_SCOPE_AGENT); }
DEV u64 ld_async(const u64* p) { u64 v; asm volatile("global_load_dwordx2 %0, %1, off sc1" : "=v"(v) : "v"(p) : "memory"); return v; }
DEV void landed(u64& v) { asm volatile("" : "+v"(v)::"memory"); }
DEV u64 mk_tag(u64 e) {
  unsigned lo = __builtin_amdgcn_readfirstlane((unsigned)e), hi = __builtin_amdgcn_readfirstlane((unsigned)(e >> 32));
  u64 x = ((u64)hi << 32) | lo;
  x = (x ^ (x >> 31)) * 0x9E3779B97F4A7C15ull + 0xD1B54A32D192ED03ull;
  return x ^ (x >> 29);
}
DEV u64 get_tag(const u64* fl) { return mk_tag(ld_agent(fl)); }
DEV void wait_flags(const u64* p, u64 tag) {
  for (int spins = 0; spins < (1 << 16); ++spins) {
    u64 v = ld_agent(p);
    if (__all(v == tag)) break;
    __builtin_amdgcn_s_sleep(2);
  }
  asm volatile("" ::: "memory");
}
DEV void publish(u64* p, u64 tag, int lane) {
  WAIT_V(0);
  if (lane == 0) st_agent(p, tag);
}

__global__ __launch_bounds__(512, 2) void fused(const char* __restrict__ xA, const char* __restrict__ Sr, const char* __restrict__ Si,
                                                const char* __restrict__ Dr, const char* __restrict__ Di, const char* __restrict__ xB,
                                                const char* __restrict__ wqkv, const char* __restrict__ wo, char* __restrict__ out,
                                                char* __restrict__ ws) {
  extern __shared__ __attribute__((aligned(16))) char shm[];
  const int bid = blockIdx.x, xcd = bid & 7, loc = bid >> 3;
  const int wid = __builtin_amdgcn_readfirstlane(threadIdx.x >> 6), lane = threadIdx.x & 63;
  u64* const fl = (u64*)(ws + WS_FLAG);
  const bool isq = loc >= 16;
  const int l16 = loc & 15;
  const int p_r = 4 * (xcd & 3) + (l16 & 3), p_c = 4 * (xcd >> 2) + (l16 >> 2);
  const int b_kq = xcd & 3, b_mt = 8 * (xcd >> 2) + (loc >> 2), b_nt = loc & 3;
  const int o_pb = 8 * (xcd & 3) + (loc & 7), o_nt = 4 * (xcd >> 2) + (loc >> 3);

  if (wid >= 4) {
    __builtin_amdgcn_s_setprio(3);
    const int lw = wid & 1;
    const int r = lane >> 3, p = lane & 7;
    const int k1 = ROT1 ? 2 * ((l16 & 3) + (l16 >> 2)) : 0;
    const int k2 = ROT2 ? 4 * b_nt + 2 * (loc >> 2) : 0;
    if (wid < 6) {
      rsrc_t rS = mk((b_kq >> 1) ? Si : Sr, 8u << 20), rWo = mk(wo, 512u << 10);
      const unsigned v_s = r * 4096 + ((p ^ (r >> 1)) * 16);
      const unsigned v_w = r * 1024 + ((p ^ (((r >> 1) & 1) * 2)) * 16);
      const unsigned s_s = (b_mt * 128 + lw * 64) * 4096 + (b_kq & 1) * 2048;
      const unsigned s_w = (lw * 64) * 1024 + o_nt * 128;
      auto issueS = [&](int k, int slot) {
        char* l = shm + slot * SLOT + lw * 8192;
        unsigned so = s_s + ((k + k2) & 15) * 128;
#pragma unroll
        for (int i = 0; i < 8; ++i) dma16<AUX_S>(rS, v_s ^ ((i & 1) << 6), so + i * 32768, l + i * 1024);
      };
      auto issueW = [&](int k, int slot) {
        char* l = shm + slot * SLOT + lw * 8192;
#pragma unroll
        for (int i = 0; i < 8; ++i) dma16<0>(rWo, v_w ^ ((i & 1) << 6), s_w + i * 8192 + k * 131072, l + i * 1024);
      };
      if (!isq) {
        rsrc_t rX = mk(xA, 2u << 20);
        const unsigned v = r * 1024 + ((p ^ ((r >> 1) & 1)) * 16);
        const unsigned s0 = (p_r * 128 + lw * 64) * 1024;
        auto issue = [&](int k) {
          char* l = shm + ((k + 2) % 5) * SLOT + lw * 8192;
          unsigned so = s0 + ((k + k1) & 7) * 128;
#pragma unroll
          for (int i = 0; i < 8; ++i) dma16<0>(rX, v ^ ((i & 3) << 5), so + i * 8192, l + i * 1024);
        };
#pragma unroll
        for (int k = 0; k < 4; ++k) issue(k);
#pragma unroll
        for (int g = 0; g < 9; ++g) { WAIT_V(24); BAR(); if (g < 4) issue(g + 4); else issueS(g - 4, g - 4); }
      } else {
        rsrc_t rX = mk(xB, 2u << 20);
        const unsigned v = r * 2048 + ((p ^ (r >> 1)) * 16);
        const int row0 = ((p_r & 7) * 128 - 16) * 2 + (p_r >> 3);
        auto issue = [&](int k) {
          char* l = shm + QBASE + ((k + 1) % 3) * QSLOT;
          unsigned ko = ((k + k1) & 7) * 128;
#pragma unroll
          for (int i = 0; i < 9; ++i) {
            int ii = lw * 9 + i;
            int row = row0 + ii * 16; if (row < 0) row += 32;
            dma16<0>(rX, v ^ ((ii & 1) << 6), row * 1024 + ko, l + ii * 1024);
          }
        };
        issue(0); issue(1); issue(2);
        WAIT_V(18); BAR();
#pragma unroll
        for (int g = 1; g < 6; ++g) { WAIT_V(9); BAR(); issue(g + 2); }
        issueS(0, 0);
        WAIT_V(17); BAR(); issueS(1, 1);
        WAIT_V(16); BAR(); issueS(2, 2);
        WAIT_V(16); BAR(); issueS(3, 3); issueS(4, 4);
      }
      int slot = 0;
#pragma unroll 1
      for (int k = 5; k < 16; ++k) { WAIT_V(24); BAR(); issueS(k, slot); slot = slot == 4 ? 0 : slot + 1; }
#pragma unroll
      for (int k = 0; k < 4; ++k) { WAIT_V(24); BAR(); issueW(k, (16 + k) % 5); }
      WAIT_V(24); BAR(); WAIT_V(16); BAR(); WAIT_V(8); BAR(); WAIT_V(0); BAR();
      if (bid == 0 && wid == 4) {
        u64 tag = get_tag(fl);
#pragma unroll 1
        for (int spins = 0; spins < (1 << 16); ++spins) {
          bool ok = true;
#pragma unroll
          for (int j = 0; j < 8; ++j) ok &= (ld_agent(fl + FL_AB + j * 64 + lane) == tag) & (ld_agent(fl + FL_O + j * 64 + lane) == tag);
          if (__all(ok)) break;
          __builtin_amdgcn_s_sleep(2);
        }
        if (lane == 0) st_agent(fl, tag);
      }
    } else {
      u64 ep = ld_async(fl), tag;
      rsrc_t rAB = mk(ws + WS_AB, 4u << 20), rO = mk(ws + WS_O, 2u << 20);
      const unsigned v_l = lane * 16;
      const unsigned s_b = ((b_kq & 1) * 128 + lw * 4) * 16384 + ((b_kq >> 1) * 512 + b_nt * 128) * 16;
      const unsigned s_o = (lw * 8) * 32768 + o_pb * 1024;
      auto issueA = [&](int k, int slot) {
        char* l = shm + slot * SLOT + 16384 + lw * 8192;
        unsigned so = s_b + ((k + k2) & 15) * 131072;
#pragma unroll
        for (int i = 0; i < 8; ++i) dma16<SC1>(rAB, v_l, so + (i >> 1) * 16384 + (i & 1) * 1024, l + i * 1024);
      };
      auto issueO = [&](int k, int slot) {
        char* l = shm + slot * SLOT + 16384 + lw * 8192;
#pragma unroll
        for (int i = 0; i < 8; ++i) dma16<SC1>(rO, v_l, s_o + (k * 16 + i) * 32768, l + i * 1024);
      };
      const u64* fab = fl + FL_AB + (((b_kq >> 1) * 4 + b_nt) * 16 + (b_kq & 1) * 8) * 4 + (lane & 31);
      if (!isq) {
        rsrc_t rD = mk((p_c >> 2) ? Di : Dr, 512u << 10);
        const unsigned v = r * 1024 + ((p ^ ((r >> 1) & 1)) * 16);
        const unsigned s0 = ((p_c & 3) * 128 + lw * 64) * 1024;
        auto issue = [&](int k) {
          char* l = shm + ((k + 2) % 5) * SLOT + 16384 + lw * 8192;
          unsigned so = s0 + ((k + k1) & 7) * 128;
#pragma unroll
          for (int i = 0; i < 8; ++i) dma16<0>(rD, v ^ ((i & 3) << 5), so + i * 8192, l + i * 1024);
        };
#pragma unroll
        for (int k = 0; k < 4; ++k) issue(k);
#pragma unroll
        for (int g = 0; g < 4; ++g) { WAIT_V(24); BAR(); issue(g + 4); }
        WAIT_V(24); BAR(); WAIT_V(16); BAR(); WAIT_V(8); BAR(); WAIT_V(0); BAR();
        LSTAMP(16);
        landed(ep); tag = mk_tag(ep);
        wait_flags(fab, tag);
        LSTAMP(17);
        issueA(0, 0); issueA(1, 1); issueA(2, 2); issueA(3, 3);
        WAIT_V(24); BAR(); issueA(4, 4);
      } else {
        rsrc_t rW = mk(wqkv, 1536u << 10);
        unsigned v[3];
#pragma unroll
        for (int i = 0; i < 3; ++i) {
          int idx = 64 * i + lane, row = idx / 24, cc = idx % 24;
          v[i] = row * 3072 + (cc >> 3) * 1024 + (((cc & 7) ^ (((row >> 1) & 1) * 2)) * 16);
        }
        const unsigned s0 = lw * 32 * 3072 + p_c * 128;
        auto issue = [&](int k) {
          char* l = shm + QBASE + ((k + 1) % 3) * QSLOT + QW + lw * 12288;
          unsigned so = s0 + ((k + k1) & 7) * 64 * 3072;
#pragma unroll
          for (int i = 0; i < 12; ++i) dma16<0>(rW, v[i % 3] ^ (((i / 3) & 1) << 6), so + (i / 3) * 8 * 3072, l + i * 1024);
        };
        issue(0); issue(1); issue(2);
        WAIT_V(24); BAR();
#pragma unroll
        for (int g = 1; g < 6; ++g) { WAIT_V(12); BAR(); issue(g + 2); }
        WAIT_V(12); BAR(); WAIT_V(0); BAR();
        LSTAMP(16);
        landed(ep); tag = mk_tag(ep);
        wait_flags(fab, tag);
        LSTAMP(17);
        issueA(0, 0); issueA(1, 1); issueA(2, 2);
        WAIT_V(16); BAR(); issueA(3, 3); issueA(4, 4);
      }
      int slot = 0;
#pragma unroll 1
      for (int k = 5; k < 12; ++k) { WAIT_V(24); BAR(); issueA(k, slot); slot = slot == 4 ? 0 : slot + 1; }
      const u64* fo = fl + FL_O + (o_pb >> 1) * 32 + (lane & 31);
      u64 of = ld_async(fo);
#pragma unroll 1
      for (int k = 12; k < 16; ++k) { WAIT_V(24); BAR(); issueA(k, slot); slot = slot == 4 ? 0 : slot + 1; }
      WAIT_V(24); BAR();
      landed(of);
      if (!__all(of == tag)) wait_flags(fo, tag);
      issueO(0, 1);
      WAIT_V(24); BAR(); issueO(1, 2);
      WAIT_V(24); BAR(); issueO(2, 3);
      WAIT_V(24); BAR(); issueO(3, 4);
      WAIT_V(24); BAR(); WAIT_V(16); BAR(); WAIT_V(8); BAR(); WAIT_V(0); BAR();
    }
    return;
  }

  const int g = lane >> 4, i = lane & 15;
  const int wr = wid >> 1, wc = wid & 1;
  u64 amp0=0, amp1=0;
  STAMP(0);
  const u64 tag = get_tag(fl);
  if (!isq) {
    const int f = ((i >> 2) << 1) | ((i >> 1) & 1), rin = 8 * (i >> 2) + (i & 3);
    unsigned aa[2], ba[2];
#pragma unroll
    for (int ks = 0; ks < 2; ++ks) {
      aa[ks] = (wr * 64 + rin) * 128 + (((4 * ks + g) ^ f) * 16);
      ba[ks] = 16384 + (wc * 64 + rin) * 128 + (((4 * ks + g) ^ f) * 16);
    }
    f32x4 acc[4][4] = {};
    bf16x8 fa[2][4], fb[2][4];
    auto rd = [&](int ks, int slot) {
      const char* l = shm + slot * SLOT;
#pragma unroll
      for (int m = 0; m < 4; ++m) fa[ks][m] = lds128(l + aa[ks] + (m >> 1) * 4096 + (m & 1) * 512);
#pragma unroll
      for (int n = 0; n < 4; ++n) fb[ks][n] = lds128(l + ba[ks] + (n >> 1) * 4096 + (n & 1) * 512);
    };
    auto mm = [&](int ks) {
#pragma unroll
      for (int m = 0; m < 4; ++m)
#pragma unroll
        for (int n = 0; n < 4; ++n) acc[m][n] = mma(fa[ks][m], fb[ks][n], acc[m][n]);
    };
    auto sched = [&]() {
#pragma unroll
      for (int q = 0; q < 8; ++q) { SGB(0x8, 1); SGB(0x100, 1); }
      SGB(0x8, 8);
    };
    int slot = 2;
    BAR(); rd(0, slot); SCHED0(); rd(1, slot); mm(0); sched(); WAIT_L0(); SCHED0();
#pragma unroll 1
    for (int k = 1; k < 8; ++k) {
      slot = slot == 4 ? 0 : slot + 1;
      BAR(); rd(0, slot); mm(1); sched(); SCHED0(); rd(1, slot); mm(0); sched(); WAIT_L0(); SCHED0();
    }
    mm(1);
    STAMP(1);
    rsrc_t rAB = mk(ws + WS_AB, 4u << 20);
    const float sg = (p_c >> 2) ? -1.f : 1.f;
    const unsigned vo = ((p_r * 16 + wr * 8 + g) * 1024 + p_c * 128 + wc * 64 + i) * 16;
#pragma unroll
    for (int P = 0; P < 2; ++P)
#pragma unroll
      for (int n = 0; n < 4; ++n)
        __builtin_amdgcn_raw_buffer_store_b128(pk8(acc[2 * P][n] * sg, acc[2 * P + 1][n] * sg), rAB, vo + P * 65536 + n * 256, 0, SC1);
    STAMP(2);
    publish(fl + FL_AB + (p_c * 16 + p_r) * 4 + wid, tag, lane);
    STAMP(3);
  } else {
    const int q4 = i >> 2, p4 = i & 3;
    unsigned xa[2];
#pragma unroll
    for (int ks = 0; ks < 2; ++ks) xa[ks] = QBASE + i * 128 + (((4 * ks + g) ^ (i >> 1)) * 16);
    const unsigned wa = QBASE + QW + (8 * g + q4) * 384 + (((2 * wid + (p4 >> 1)) ^ (2 * (((g & 1) << 1) | (q4 >> 1)))) * 16) + 8 * (p4 & 1);
    f32x4 aq[8] = {}, ak[9] = {}, av[9] = {};
    bf16x8 fx[2][9], fw[2][3];
    auto rd = [&](int ks, int slot) {
      const char* l = shm + slot * QSLOT;
#pragma unroll
      for (int s = 0; s < 3; ++s) fw[ks][s] = cat(ldstr(l + wa + ks * 12288 + s * 128), ldstr(l + wa + ks * 12288 + 1536 + s * 128));
#pragma unroll
      for (int b = 0; b < 9; ++b) fx[ks][b] = lds128(l + xa[ks] + b * 2048);
    };
    auto mm = [&](int ks) {
#pragma unroll
      for (int b = 0; b < 9; ++b) {
        if (b) aq[b - 1] = mma(fw[ks][0], fx[ks][b], aq[b - 1]);
        ak[b] = mma(fw[ks][1], fx[ks][b], ak[b]);
        av[b] = mma(fx[ks][b], fw[ks][2], av[b]);
      }
    };
    auto sched = [&]() {
#pragma unroll
      for (int q = 0; q < 15; ++q) { SGB(0x8, 1); SGB(0x100, 1); }
      SGB(0x8, 11);
    };
    int slot = 1;
    BAR(); rd(0, slot); SCHED0(); rd(1, slot); mm(0); sched(); WAIT_L0(); SCHED0();
#pragma unroll 1
    for (int k = 1; k < 8; ++k) {
      slot = slot == 2 ? 0 : slot + 1;
      BAR(); rd(0, slot); mm(1); sched(); SCHED0(); rd(1, slot); mm(0); sched(); WAIT_L0(); SCHED0();
    }
    mm(1);
    STAMP(1);
    bf16x4 kf[9], vf[9];
#pragma unroll
    for (int b = 0; b < 9; ++b) { kf[b] = pk4(ak[b]); vf[b] = pk4(av[b]); }
    constexpr float NEG = -3.0e38f;
    f32x4 cp, cc, c0;
    u32x2 mp, mc;
#pragma unroll
    for (int r = 0; r < 4; ++r) {
      int key = 4 * g + r;
      bool vp = key >= i + 9, vc = (key <= i) && (key >= i - 7);
      cp[r] = vp ? 0.f : NEG; cc[r] = vc ? 0.f : NEG;
      c0[r] = ((p_r & 7) == 0) ? NEG : cp[r];
      if (!(r & 1)) { mp[r >> 1] = vp ? 0xffffu : 0u; mc[r >> 1] = vc ? 0xffffu : 0u; }
      else { mp[r >> 1] |= vp ? 0xffff0000u : 0u; mc[r >> 1] |= vc ? 0xffff0000u : 0u; }
    }
    const bf16x4 ones = {0x3f80, 0x3f80, 0x3f80, 0x3f80};
    constexpr float CE = 0.25f * 1.4426950408889634f;
    u32x2 ob[8];
#pragma unroll
    for (int b = 0; b < 8; ++b) {
      bf16x4 qf = pk4(aq[b]);
      f32x4 sp = mma16(kf[b], qf, b == 0 ? c0 : cp), sc = mma16(kf[b + 1], qf, cc);
      f32x4 s = {fmaxf(sp[0], sc[0]), fmaxf(sp[1], sc[1]), fmaxf(sp[2], sc[2]), fmaxf(sp[3], sc[3])};
      u32x2 sb = pk4u(s);
      float l0 = blo(sb[0]), l1 = bhi(sb[0]), l2 = blo(sb[1]), l3 = bhi(sb[1]);
      float mx = red4max(vmax(vmax3(l0, l1, l2), l3));
      float mc2 = mx * CE;
      f32x4 e = {__builtin_amdgcn_exp2f(l0 * CE - mc2), __builtin_amdgcn_exp2f(l1 * CE - mc2), __builtin_amdgcn_exp2f(l2 * CE - mc2),
                 __builtin_amdgcn_exp2f(l3 * CE - mc2)};
      u32x2 eb = pk4u(e);
      f32x4 z = {};
      f32x4 sum = mma16(ones, __builtin_bit_cast(bf16x4, eb), z);
      f32x4 o = mma16(vf[b], __builtin_bit_cast(bf16x4, eb & mp), z);
      o = mma16(vf[b + 1], __builtin_bit_cast(bf16x4, eb & mc), o);
      float inv = __builtin_amdgcn_rcpf(sum[0]);
      ob[b] = pk4u(o * inv);
    }
    rsrc_t rO = mk(ws + WS_O, 2u << 20);
    const unsigned vo = (((p_c * 4 + wid) * 2 + (g >> 1)) * 2048 + p_r * 128 + (g & 1) * 16 + i) * 16;
#pragma unroll
    for (int P = 0; P < 4; ++P) __builtin_amdgcn_raw_buffer_store_b128(swap16(ob[2 * P], ob[2 * P + 1]), rO, vo + P * 512, 0, SC1);
    STAMP(2);
    publish(fl + FL_O + (p_r * 8 + p_c) * 4 + wid, tag, lane);
    STAMP(3);
  }
  f32x4 keep[4];
  {
    unsigned sb[2];
#pragma unroll
    for (int ks = 0; ks < 2; ++ks) sb[ks] = (wc * 64 + i) * 128 + (((4 * ks + g) ^ (i >> 1)) * 16);
    const unsigned ab = 16384 + g * 2048 + (wr * 64 + i) * 16;
    f32x4 acc[4][4] = {};
    bf16x8 fa[2][4], fb[2][4];
    auto rd = [&](int ks, int slot) {
      const char* l = shm + slot * SLOT;
#pragma unroll
      for (int m = 0; m < 4; ++m) fa[ks][m] = lds128(l + ab + ks * 8192 + m * 256);
#pragma unroll
      for (int n = 0; n < 4; ++n) fb[ks][n] = lds128(l + sb[ks] + n * 2048);
    };
    auto mm = [&](int ks) {
#pragma unroll
      for (int m = 0; m < 4; ++m)
#pragma unroll
        for (int n = 0; n < 4; ++n) acc[m][n] = mma(fa[ks][m], fb[ks][n], acc[m][n]);
    };
    auto sched = [&]() {
#pragma unroll
      for (int q = 0; q < 8; ++q) { SGB(0x8, 1); SGB(0x100, 1); }
      SGB(0x8, 8);
    };
    int slot = 0;
    BAR();
    STAMP(4);
    rd(0, slot); SCHED0(); rd(1, slot); mm(0); sched(); WAIT_L0(); SCHED0();
#pragma unroll 1
    for (int k = 1; k < 16; ++k) {
      slot = slot == 4 ? 0 : slot + 1;
      BAR(); rd(0, slot); mm(1); sched(); SCHED0(); rd(1, slot); mm(0); sched(); WAIT_L0(); SCHED0();
    }
    mm(1);
    STAMP(5);
    rsrc_t rP = mk(ws + WS_PART, 8u << 20);
    const unsigned pb = (b_mt * 4 + b_nt) * 131072 + b_kq * 8192 + wid * 2048;
#pragma unroll
    for (int n = 0; n < 4; ++n) {
      if (n != b_kq) {
#pragma unroll
        for (int P = 0; P < 2; ++P)
          __builtin_amdgcn_raw_buffer_store_b128(pk8(acc[2 * P][n], acc[2 * P + 1][n]), rP, pb + n * 32768 + lane * 16 + P * 1024, 0, SC1);
      } else {
#pragma unroll
        for (int m = 0; m < 4; ++m) keep[m] = acc[m][n];
      }
    }
    STAMP(6);
  }
  {
    const int q4 = i >> 2, p4 = i & 3;
    const int sw = 2 * (((g & 1) << 1) | (q4 >> 1));
    unsigned wa[2];
#pragma unroll
    for (int a = 0; a < 2; ++a) wa[a] = (8 * g + q4) * 128 + (((wr * 4 + 2 * a + (p4 >> 1)) ^ sw) * 16) + 8 * (p4 & 1);
    const unsigned oa = 16384 + g * 1024 + (wc * 32 + i) * 16;
    f32x4 acc[2][2] = {};
    bf16x8 fa[4][2], fb[4][2];
    auto rd = [&](int slot) {
      const char* l = shm + slot * SLOT;
#pragma unroll
      for (int ks = 0; ks < 4; ++ks) {
#pragma unroll
        for (int a = 0; a < 2; ++a) fa[ks][a] = cat(ldstr(l + wa[a] + ks * 4096), ldstr(l + wa[a] + ks * 4096 + 512));
#pragma unroll
        for (int b = 0; b < 2; ++b) fb[ks][b] = lds128(l + oa + ks * 4096 + b * 256);
      }
    };
    auto mm = [&]() {
#pragma unroll
      for (int ks = 0; ks < 4; ++ks)
#pragma unroll
        for (int a = 0; a < 2; ++a)
#pragma unroll
          for (int b = 0; b < 2; ++b) acc[a][b] = mma(fa[ks][a], fb[ks][b], acc[a][b]);
    };
    BAR(); rd(1); mm(); SCHED0();
    publish(fl + FL_P + ((b_mt * 4 + b_nt) * 4 + wid) * 4 + b_kq, tag, lane);
    STAMP(7);
#pragma unroll 1
    for (int k = 1; k < 4; ++k) { BAR(); rd(1 + k); mm(); SCHED0(); }
    STAMP(8);
    rsrc_t rOut = mk(out, 4u << 20);
#pragma unroll
    for (int b = 0; b < 2; ++b) {
      u32x4 v = swap16(pk4u(acc[0][b]), pk4u(acc[1][b]));
      int pos = o_pb * 64 + wc * 32 + b * 16 + i, t = 2 * (pos & 1023) + (pos >> 10);
      int n = o_nt * 64 + wr * 32 + 16 * (g & 1) + 8 * (g >> 1);
      __builtin_amdgcn_raw_buffer_store_b128(v, rOut, t * 2048 + 1024 + n * 2, 0, 0);
    }
  }
  {
    wait_flags(fl + FL_P + ((b_mt * 4 + b_nt) * 4 + wid) * 4 + (lane & 3), tag);
    STAMP(9);
    rsrc_t rP = mk(ws + WS_PART, 8u << 20);
    const unsigned pb = (b_mt * 4 + b_nt) * 131072 + b_kq * 32768 + wid * 2048;
    u32x4 v[3][2];
#pragma unroll
    for (int s = 0; s < 3; ++s)
#pragma unroll
      for (int P = 0; P < 2; ++P)
        v[s][P] = __builtin_amdgcn_raw_buffer_load_b128(rP, lane * 16 + P * 1024, pb + ((b_kq + 1 + s) & 3) * 8192, SC1);
    rsrc_t rOut = mk(out, 4u << 20);
    int t = b_mt * 128 + wc * 64 + b_kq * 16 + i;
#pragma unroll
    for (int P = 0; P < 2; ++P) {
      f32x4 lo = keep[2 * P], hi = keep[2 * P + 1];
#pragma unroll
      for (int s = 0; s < 3; ++s) {
        f32x4 a = {blo(v[s][P][0]), bhi(v[s][P][0]), blo(v[s][P][1]), bhi(v[s][P][1])};
        f32x4 b = {blo(v[s][P][2]), bhi(v[s][P][2]), blo(v[s][P][3]), bhi(v[s][P][3])};
        lo += a; hi += b;
      }
      int e = b_nt * 128 + wr * 64 + 32 * P + 8 * g;
      __builtin_amdgcn_raw_buffer_store_b128(pk8(lo, hi), rOut, t * 2048 + e * 2, 0, 0);
    }
    STAMP(10);
  }
}

extern "C" void kernel_launch(void* const* d_in, const int* in_sizes, int n_in, void* d_out, int out_size, void* d_ws, size_t ws_size,
                              hipStream_t stream) {
  (void)hipFuncSetAttribute((const void*)fused, hipFuncAttributeMaxDynamicSharedMemorySize, LDS_BYTES);
  fused<<<256, 512, LDS_BYTES, stream>>>((const char*)d_in[0], (const char*)d_in[1], (const char*)d_in[2], (const char*)d_in[3],
                                         (const char*)d_in[4], (const char*)d_in[5], (const char*)d_in[6], (const char*)d_in[7],
                                         (char*)d_out, (char*)d_ws);
}
```

```cpp
#define AMP 10
#define AMPB 0
#include <hip/hip_runtime.h>
#include <hip/hip_bf16.h>
#include <utility>

typedef __attribute__((ext_vector_type(8))) short bf16x8;
typedef __attribute__((ext_vector_type(4))) short bf16x4;
typedef __attribute__((ext_vector_type(4))) float f32x4;
typedef __attribute__((ext_vector_type(2))) float f32x2;
typedef __attribute__((ext_vector_type(2))) __bf16 bf2;
typedef __attribute__((ext_vector_type(4))) unsigned u32x4;
typedef __attribute__((ext_vector_type(2))) unsigned u32x2;
typedef unsigned long long u64;
typedef __amdgpu_buffer_rsrc_t rsrc_t;
#define LAS __attribute__((address_space(3)))
#define GAS __attribute__((address_space(1)))
#define DEV __device__ __forceinline__

#define WAIT_V(n) asm volatile("s_waitcnt vmcnt(%0)" ::"n"(n) : "memory")
#define WAIT_L0() asm volatile("s_waitcnt lgkmcnt(0)" ::: "memory")
#define BAR() do{asm volatile("" ::: "memory"); __builtin_amdgcn_s_barrier(); asm volatile("" ::: "memory");}while(0)
#define SCHED0() __builtin_amdgcn_sched_barrier(0)
#define SGB(m, n) __builtin_amdgcn_sched_group_barrier(m, n, 0)

constexpr int SLOT = 32768, LDS_BYTES = 163840;
constexpr int QSLOT = 43008, QBASE = LDS_BYTES - 3 * QSLOT, QW = 18432;
constexpr unsigned WS_AB = 0, WS_O = 4u << 20, WS_FLAG = 6u << 20, WS_PART = 8u << 20;
constexpr unsigned FL_AB = 128, FL_O = 1024, FL_P = 2048;
constexpr int SC1 = 16;

#define AUX_S 0
#define ROT1 1
#define ROT2 1

#define STAMP(n) do{ if((n)==0) amp0=wall_clock64(); if((n)==AMP) amp1=wall_clock64(); if((n)==10 && blockIdx.x==AMPB && wid==0){ amp1=amp0; amp0=ld_agent((const u64*)(ws+(200u<<20))); u64 tn=wall_clock64(); while(wall_clock64()-tn < 10*(amp1-amp0)) __builtin_amdgcn_s_sleep(1);} }while(0)
#define LSTAMP(n)

template <int AUX> DEV void dma16(rsrc_t r, unsigned voff, unsigned soff, char* lds) {
  __builtin_amdgcn_raw_ptr_buffer_load_lds(r, (LAS void*)lds, 16, voff, soff, 0, AUX);
}
DEV rsrc_t mk(const void* p, unsigned bytes) { return __builtin_amdgcn_make_buffer_rsrc((void*)p, 0, bytes, 0x00020000); }
DEV unsigned pk(float a, float b) { f32x2 v = {a, b}; return __builtin_bit_cast(unsigned, __builtin_convertvector(v, bf2)); }
DEV u32x2 pk4u(f32x4 v) { u32x2 r = {pk(v[0], v[1]), pk(v[2], v[3])}; return r; }
DEV bf16x4 pk4(f32x4 v) { return __builtin_bit_cast(bf16x4, pk4u(v)); }
DEV u32x4 pk8(f32x4 lo, f32x4 hi) { u32x4 v = {pk(lo[0], lo[1]), pk(lo[2], lo[3]), pk(hi[0], hi[1]), pk(hi[2], hi[3])}; return v; }
DEV float blo(unsigned u) { return __builtin_bit_cast(float, u << 16); }
DEV float bhi(unsigned u) { return __builtin_bit_cast(float, u & 0xffff0000u); }
DEV bf16x8 lds128(const char* p) { return *(const bf16x8*)p; }
DEV bf16x4 ldstr(const char* p) { return __builtin_amdgcn_ds_read_tr16_b64_v4i16((LAS bf16x4*)p); }
DEV bf16x8 cat(bf16x4 a, bf16x4 b) { return __builtin_shufflevector(a, b, 0, 1, 2, 3, 4, 5, 6, 7); }
DEV f32x4 mma(bf16x8 a, bf16x8 b, f32x4 c) { return __builtin_amdgcn_mfma_f32_16x16x32_bf16(a, b, c, 0, 0, 0); }
DEV f32x4 mma16(bf16x4 a, bf16x4 b, f32x4 c) { return __builtin_amdgcn_mfma_f32_16x16x16bf16_1k(a, b, c, 0, 0, 0); }
DEV float vmax(float a, float b) { float r; asm("v_max_f32 %0, %1, %2" : "=v"(r) : "v"(a), "v"(b)); return r; }
DEV float vmax3(float a, float b, float c) { float r; asm("v_max3_f32 %0, %1, %2, %3" : "=v"(r) : "v"(a), "v"(b), "v"(c)); return r; }
DEV unsigned cpy(unsigned u) { asm("" : "+v"(u)); return u; }
DEV float red4max(float x) {
  unsigned u = __builtin_bit_cast(unsigned, x);
  auto a = __builtin_amdgcn_permlane32_swap(u, cpy(u), false, false);
  unsigned a0 = a[0], a1 = a[1];
  x = vmax(__builtin_bit_cast(float, a0), __builtin_bit_cast(float, a1));
  u = __builtin_bit_cast(unsigned, x);
  auto b = __builtin_amdgcn_permlane16_swap(u, cpy(u), false, false);
  unsigned b0 = b[0], b1 = b[1];
  return vmax(__builtin_bit_cast(float, b0), __builtin_bit_cast(float, b1));
}
DEV u32x4 swap16(u32x2 x, u32x2 y) {
  auto a = __builtin_amdgcn_permlane16_swap(x[0], y[0], false, false);
  auto b = __builtin_amdgcn_permlane16_swap(x[1], y[1], false, false);
  unsigned a0 = a[0], a1 = a[1], b0 = b[0], b1 = b[1];
  u32x4 r = {a0, b0, a1, b1};
  return r;
}
DEV u64 ld_agent(const u64* p) { return __hip_atomic_load((GAS const u64*)p, __ATOMIC_RELAXED, __HIP_MEMORY_SCOPE_AGENT); }
DEV void st_agent(u64* p, u64 v) { __hip_atomic_store((GAS u64*)p, v, __ATOMIC_RELAXED, __HIP_MEMORY_SCOPE_AGENT); }
DEV u64 ld_async(const u64* p) { u64 v; asm volatile("global_load_dwordx2 %0, %1, off sc1" : "=v"(v) : "v"(p) : "memory"); return v; }
DEV void landed(u64& v) { asm volatile("" : "+v"(v)::"memory"); }
DEV u64 mk_tag(u64 e) {
  unsigned lo = __builtin_amdgcn_readfirstlane((unsigned)e), hi = __builtin_amdgcn_readfirstlane((unsigned)(e >> 32));
  u64 x = ((u64)hi << 32) | lo;
  x = (x ^ (x >> 31)) * 0x9E3779B97F4A7C15ull + 0xD1B54A32D192ED03ull;
  return x ^ (x >> 29);
}
DEV u64 get_tag(const u64* fl) { return mk_tag(ld_agent(fl)); }
DEV void wait_flags(const u64* p, u64 tag) {
  for (int spins = 0; spins < (1 << 16); ++spins) {
    u64 v = ld_agent(p);
    if (__all(v == tag)) break;
    __builtin_amdgcn_s_sleep(2);
  }
  asm volatile("" ::: "memory");
}
DEV void publish(u64* p, u64 tag, int lane) {
  WAIT_V(0);
  if (lane == 0) st_agent(p, tag);
}

__global__ __launch_bounds__(512, 2) void fused(const char* __restrict__ xA, const char* __restrict__ Sr, const char* __restrict__ Si,
                                                const char* __restrict__ Dr, const char* __restrict__ Di, const char* __restrict__ xB,
                                                const char* __restrict__ wqkv, const char* __restrict__ wo, char* __restrict__ out,
                                                char* __restrict__ ws) {
  extern __shared__ __attribute__((aligned(16))) char shm[];
  const int bid = blockIdx.x, xcd = bid & 7, loc = bid >> 3;
  const int wid = __builtin_amdgcn_readfirstlane(threadIdx.x >> 6), lane = threadIdx.x & 63;
  u64* const fl = (u64*)(ws + WS_FLAG);
  const bool isq = loc >= 16;
  const int l16 = loc & 15;
  const int p_r = 4 * (xcd & 3) + (l16 & 3), p_c = 4 * (xcd >> 2) + (l16 >> 2);
  const int b_kq = xcd & 3, b_mt = 8 * (xcd >> 2) + (loc >> 2), b_nt = loc & 3;
  const int o_pb = 8 * (xcd & 3) + (loc & 7), o_nt = 4 * (xcd >> 2) + (loc >> 3);

  if (wid >= 4) {
    __builtin_amdgcn_s_setprio(3);
    const int lw = wid & 1;
    const int r = lane >> 3, p = lane & 7;
    const int k1 = ROT1 ? 2 * ((l16 & 3) + (l16 >> 2)) : 0;
    const int k2 = ROT2 ? 4 * b_nt + 2 * (loc >> 2) : 0;
    if (wid < 6) {
      rsrc_t rS = mk((b_kq >> 1) ? Si : Sr, 8u << 20), rWo = mk(wo, 512u << 10);
      const unsigned v_s = r * 4096 + ((p ^ (r >> 1)) * 16);
      const unsigned v_w = r * 1024 + ((p ^ (((r >> 1) & 1) * 2)) * 16);
      const unsigned s_s = (b_mt * 128 + lw * 64) * 4096 + (b_kq & 1) * 2048;
      const unsigned s_w = (lw * 64) * 1024 + o_nt * 128;
      auto issueS = [&](int k, int slot) {
        char* l = shm + slot * SLOT + lw * 8192;
        unsigned so = s_s + ((k + k2) & 15) * 128;
#pragma unroll
        for (int i = 0; i < 8; ++i) dma16<AUX_S>(rS, v_s ^ ((i & 1) << 6), so + i * 32768, l + i * 1024);
      };
      auto issueW = [&](int k, int slot) {
        char* l = shm + slot * SLOT + lw * 8192;
#pragma unroll
        for (int i = 0; i < 8; ++i) dma16<0>(rWo, v_w ^ ((i & 1) << 6), s_w + i * 8192 + k * 131072, l + i * 1024);
      };
      if (!isq) {
        rsrc_t rX = mk(xA, 2u << 20);
        const unsigned v = r * 1024 + ((p ^ ((r >> 1) & 1)) * 16);
        const unsigned s0 = (p_r * 128 + lw * 64) * 1024;
        auto issue = [&](int k) {
          char* l = shm + ((k + 2) % 5) * SLOT + lw * 8192;
          unsigned so = s0 + ((k + k1) & 7) * 128;
#pragma unroll
          for (int i = 0; i < 8; ++i) dma16<0>(rX, v ^ ((i & 3) << 5), so + i * 8192, l + i * 1024);
        };
#pragma unroll
        for (int k = 0; k < 4; ++k) issue(k);
#pragma unroll
        for (int g = 0; g < 9; ++g) { WAIT_V(24); BAR(); if (g < 4) issue(g + 4); else issueS(g - 4, g - 4); }
      } else {
        rsrc_t rX = mk(xB, 2u << 20);
        const unsigned v = r * 2048 + ((p ^ (r >> 1)) * 16);
        const int row0 = ((p_r & 7) * 128 - 16) * 2 + (p_r >> 3);
        auto issue = [&](int k) {
          char* l = shm + QBASE + ((k + 1) % 3) * QSLOT;
          unsigned ko = ((k + k1) & 7) * 128;
#pragma unroll
          for (int i = 0; i < 9; ++i) {
            int ii = lw * 9 + i;
            int row = row0 + ii * 16; if (row < 0) row += 32;
            dma16<0>(rX, v ^ ((ii & 1) << 6), row * 1024 + ko, l + ii * 1024);
          }
        };
        issue(0); issue(1); issue(2);
        WAIT_V(18); BAR();
#pragma unroll
        for (int g = 1; g < 6; ++g) { WAIT_V(9); BAR(); issue(g + 2); }
        issueS(0, 0);
        WAIT_V(17); BAR(); issueS(1, 1);
        WAIT_V(16); BAR(); issueS(2, 2);
        WAIT_V(16); BAR(); issueS(3, 3); issueS(4, 4);
      }
      int slot = 0;
#pragma unroll 1
      for (int k = 5; k < 16; ++k) { WAIT_V(24); BAR(); issueS(k, slot); slot = slot == 4 ? 0 : slot + 1; }
#pragma unroll
      for (int k = 0; k < 4; ++k) { WAIT_V(24); BAR(); issueW(k, (16 + k) % 5); }
      WAIT_V(24); BAR(); WAIT_V(16); BAR(); WAIT_V(8); BAR(); WAIT_V(0); BAR();
      if (bid == 0 && wid == 4) {
        u64 tag = get_tag(fl);
#pragma unroll 1
        for (int spins = 0; spins < (1 << 16); ++spins) {
          bool ok = true;
#pragma unroll
          for (int j = 0; j < 8; ++j) ok &= (ld_agent(fl + FL_AB + j * 64 + lane) == tag) & (ld_agent(fl + FL_O + j * 64 + lane) == tag);
          if (__all(ok)) break;
          __builtin_amdgcn_s_sleep(2);
        }
        if (lane == 0) st_agent(fl, tag);
      }
    } else {
      u64 ep = ld_async(fl), tag;
      rsrc_t rAB = mk(ws + WS_AB, 4u << 20), rO = mk(ws + WS_O, 2u << 20);
      const unsigned v_l = lane * 16;
      const unsigned s_b = ((b_kq & 1) * 128 + lw * 4) * 16384 + ((b_kq >> 1) * 512 + b_nt * 128) * 16;
      const unsigned s_o = (lw * 8) * 32768 + o_pb * 1024;
      auto issueA = [&](int k, int slot) {
        char* l = shm + slot * SLOT + 16384 + lw * 8192;
        unsigned so = s_b + ((k + k2) & 15) * 131072;
#pragma unroll
        for (int i = 0; i < 8; ++i) dma16<SC1>(rAB, v_l, so + (i >> 1) * 16384 + (i & 1) * 1024, l + i * 1024);
      };
      auto issueO = [&](int k, int slot) {
        char* l = shm + slot * SLOT + 16384 + lw * 8192;
#pragma unroll
        for (int i = 0; i < 8; ++i) dma16<SC1>(rO, v_l, s_o + (k * 16 + i) * 32768, l + i * 1024);
      };
      const u64* fab = fl + FL_AB + (((b_kq >> 1) * 4 + b_nt) * 16 + (b_kq & 1) * 8) * 4 + (lane & 31);
      if (!isq) {
        rsrc_t rD = mk((p_c >> 2) ? Di : Dr, 512u << 10);
        const unsigned v = r * 1024 + ((p ^ ((r >> 1) & 1)) * 16);
        const unsigned s0 = ((p_c & 3) * 128 + lw * 64) * 1024;
        auto issue = [&](int k) {
          char* l = shm + ((k + 2) % 5) * SLOT + 16384 + lw * 8192;
          unsigned so = s0 + ((k + k1) & 7) * 128;
#pragma unroll
          for (int i = 0; i < 8; ++i) dma16<0>(rD, v ^ ((i & 3) << 5), so + i * 8192, l + i * 1024);
        };
#pragma unroll
        for (int k = 0; k < 4; ++k) issue(k);
#pragma unroll
        for (int g = 0; g < 4; ++g) { WAIT_V(24); BAR(); issue(g + 4); }
        WAIT_V(24); BAR(); WAIT_V(16); BAR(); WAIT_V(8); BAR(); WAIT_V(0); BAR();
        LSTAMP(16);
        landed(ep); tag = mk_tag(ep);
        wait_flags(fab, tag);
        LSTAMP(17);
        issueA(0, 0); issueA(1, 1); issueA(2, 2); issueA(3, 3);
        WAIT_V(24); BAR(); issueA(4, 4);
      } else {
        rsrc_t rW = mk(wqkv, 1536u << 10);
        unsigned v[3];
#pragma unroll
        for (int i = 0; i < 3; ++i) {
          int idx = 64 * i + lane, row = idx / 24, cc = idx % 24;
          v[i] = row * 3072 + (cc >> 3) * 1024 + (((cc & 7) ^ (((row >> 1) & 1) * 2)) * 16);
        }
        const unsigned s0 = lw * 32 * 3072 + p_c * 128;
        auto issue = [&](int k) {
          char* l = shm + QBASE + ((k + 1) % 3) * QSLOT + QW + lw * 12288;
          unsigned so = s0 + ((k + k1) & 7) * 64 * 3072;
#pragma unroll
          for (int i = 0; i < 12; ++i) dma16<0>(rW, v[i % 3] ^ (((i / 3) & 1) << 6), so + (i / 3) * 8 * 3072, l + i * 1024);
        };
        issue(0); issue(1); issue(2);
        WAIT_V(24); BAR();
#pragma unroll
        for (int g = 1; g < 6; ++g) { WAIT_V(12); BAR(); issue(g + 2); }
        WAIT_V(12); BAR(); WAIT_V(0); BAR();
        LSTAMP(16);
        landed(ep); tag = mk_tag(ep);
        wait_flags(fab, tag);
        LSTAMP(17);
        issueA(0, 0); issueA(1, 1); issueA(2, 2);
        WAIT_V(16); BAR(); issueA(3, 3); issueA(4, 4);
      }
      int slot = 0;
#pragma unroll 1
      for (int k = 5; k < 12; ++k) { WAIT_V(24); BAR(); issueA(k, slot); slot = slot == 4 ? 0 : slot + 1; }
      const u64* fo = fl + FL_O + (o_pb >> 1) * 32 + (lane & 31);
      u64 of = ld_async(fo);
#pragma unroll 1
      for (int k = 12; k < 16; ++k) { WAIT_V(24); BAR(); issueA(k, slot); slot = slot == 4 ? 0 : slot + 1; }
      WAIT_V(24); BAR();
      landed(of);
      if (!__all(of == tag)) wait_flags(fo, tag);
      issueO(0, 1);
      WAIT_V(24); BAR(); issueO(1, 2);
      WAIT_V(24); BAR(); issueO(2, 3);
      WAIT_V(24); BAR(); issueO(3, 4);
      WAIT_V(24); BAR(); WAIT_V(16); BAR(); WAIT_V(8); BAR(); WAIT_V(0); BAR();
    }
    return;
  }

  const int g = lane >> 4, i = lane & 15;
  const int wr = wid >> 1, wc = wid & 1;
  u64 amp0=0, amp1=0;
  STAMP(0);
  const u64 tag = get_tag(fl);
  if (!isq) {
    const int f = ((i >> 2) << 1) | ((i >> 1) & 1), rin = 8 * (i >> 2) + (i & 3);
    unsigned aa[2], ba[2];
#pragma unroll
    for (int ks = 0; ks < 2; ++ks) {
      aa[ks] = (wr * 64 + rin) * 128 + (((4 * ks + g) ^ f) * 16);
      ba[ks] = 16384 + (wc * 64 + rin) * 128 + (((4 * ks + g) ^ f) * 16);
    }
    f32x4 acc[4][4] = {};
    bf16x8 fa[2][4], fb[2][4];
    auto rd = [&](int ks, int slot) {
      const char* l = shm + slot * SLOT;
#pragma unroll
      for (int m = 0; m < 4; ++m) fa[ks][m] = lds128(l + aa[ks] + (m >> 1) * 4096 + (m & 1) * 512);
#pragma unroll
      for (int n = 0; n < 4; ++n) fb[ks][n] = lds128(l + ba[ks] + (n >> 1) * 4096 + (n & 1) * 512);
    };
    auto mm = [&](int ks) {
#pragma unroll
      for (int m = 0; m < 4; ++m)
#pragma unroll
        for (int n = 0; n < 4; ++n) acc[m][n] = mma(fa[ks][m], fb[ks][n], acc[m][n]);
    };
    auto sched = [&]() {
#pragma unroll
      for (int q = 0; q < 8; ++q) { SGB(0x8, 1); SGB(0x100, 1); }
      SGB(0x8, 8);
    };
    int slot = 2;
    BAR(); rd(0, slot); SCHED0(); rd(1, slot); mm(0); sched(); WAIT_L0(); SCHED0();
#pragma unroll 1
    for (int k = 1; k < 8; ++k) {
      slot = slot == 4 ? 0 : slot + 1;
      BAR(); rd(0, slot); mm(1); sched(); SCHED0(); rd(1, slot); mm(0); sched(); WAIT_L0(); SCHED0();
    }
    mm(1);
    STAMP(1);
    rsrc_t rAB = mk(ws + WS_AB, 4u << 20);
    const float sg = (p_c >> 2) ? -1.f : 1.f;
    const unsigned vo = ((p_r * 16 + wr * 8 + g) * 1024 + p_c * 128 + wc * 64 + i) * 16;
#pragma unroll
    for (int P = 0; P < 2; ++P)
#pragma unroll
      for (int n = 0; n < 4; ++n)
        __builtin_amdgcn_raw_buffer_store_b128(pk8(acc[2 * P][n] * sg, acc[2 * P + 1][n] * sg), rAB, vo + P * 65536 + n * 256, 0, SC1);
    STAMP(2);
    publish(fl + FL_AB + (p_c * 16 + p_r) * 4 + wid, tag, lane);
    STAMP(3);
  } else {
    const int q4 = i >> 2, p4 = i & 3;
    unsigned xa[2];
#pragma unroll
    for (int ks = 0; ks < 2; ++ks) xa[ks] = QBASE + i * 128 + (((4 * ks + g) ^ (i >> 1)) * 16);
    const unsigned wa = QBASE + QW + (8 * g + q4) * 384 + (((2 * wid + (p4 >> 1)) ^ (2 * (((g & 1) << 1) | (q4 >> 1)))) * 16) + 8 * (p4 & 1);
    f32x4 aq[8] = {}, ak[9] = {}, av[9] = {};
    bf16x8 fx[2][9], fw[2][3];
    auto rd = [&](int ks, int slot) {
      const char* l = shm + slot * QSLOT;
#pragma unroll
      for (int s = 0; s < 3; ++s) fw[ks][s] = cat(ldstr(l + wa + ks * 12288 + s * 128), ldstr(l + wa + ks * 12288 + 1536 + s * 128));
#pragma unroll
      for (int b = 0; b < 9; ++b) fx[ks][b] = lds128(l + xa[ks] + b * 2048);
    };
    auto mm = [&](int ks) {
#pragma unroll
      for (int b = 0; b < 9; ++b) {
        if (b) aq[b - 1] = mma(fw[ks][0], fx[ks][b], aq[b - 1]);
        ak[b] = mma(fw[ks][1], fx[ks][b], ak[b]);
        av[b] = mma(fx[ks][b], fw[ks][2], av[b]);
      }
    };
    auto sched = [&]() {
#pragma unroll
      for (int q = 0; q < 15; ++q) { SGB(0x8, 1); SGB(0x100, 1); }
      SGB(0x8, 11);
    };
    int slot = 1;
    BAR(); rd(0, slot); SCHED0(); rd(1, slot); mm(0); sched(); WAIT_L0(); SCHED0();
#pragma unroll 1
    for (int k = 1; k < 8; ++k) {
      slot = slot == 2 ? 0 : slot + 1;
      BAR(); rd(0, slot); mm(1); sched(); SCHED0(); rd(1, slot); mm(0); sched(); WAIT_L0(); SCHED0();
    }
    mm(1);
    STAMP(1);
    bf16x4 kf[9], vf[9];
#pragma unroll
    for (int b = 0; b < 9; ++b) { kf[b] = pk4(ak[b]); vf[b] = pk4(av[b]); }
    constexpr float NEG = -3.0e38f;
    f32x4 cp, cc, c0;
    u32x2 mp, mc;
#pragma unroll
    for (int r = 0; r < 4; ++r) {
      int key = 4 * g + r;
      bool vp = key >= i + 9, vc = (key <= i) && (key >= i - 7);
      cp[r] = vp ? 0.f : NEG; cc[r] = vc ? 0.f : NEG;
      c0[r] = ((p_r & 7) == 0) ? NEG : cp[r];
      if (!(r & 1)) { mp[r >> 1] = vp ? 0xffffu : 0u; mc[r >> 1] = vc ? 0xffffu : 0u; }
      else { mp[r >> 1] |= vp ? 0xffff0000u : 0u; mc[r >> 1] |= vc ? 0xffff0000u : 0u; }
    }
    const bf16x4 ones = {0x3f80, 0x3f80, 0x3f80, 0x3f80};
    constexpr float CE = 0.25f * 1.4426950408889634f;
    u32x2 ob[8];
#pragma unroll
    for (int b = 0; b < 8; ++b) {
      bf16x4 qf = pk4(aq[b]);
      f32x4 sp = mma16(kf[b], qf, b == 0 ? c0 : cp), sc = mma16(kf[b + 1], qf, cc);
      f32x4 s = {fmaxf(sp[0], sc[0]), fmaxf(sp[1], sc[1]), fmaxf(sp[2], sc[2]), fmaxf(sp[3], sc[3])};
      u32x2 sb = pk4u(s);
      float l0 = blo(sb[0]), l1 = bhi(sb[0]), l2 = blo(sb[1]), l3 = bhi(sb[1]);
      float mx = red4max(vmax(vmax3(l0, l1, l2), l3));
      float mc2 = mx * CE;
      f32x4 e = {__builtin_amdgcn_exp2f(l0 * CE - mc2), __builtin_amdgcn_exp2f(l1 * CE - mc2), __builtin_amdgcn_exp2f(l2 * CE - mc2),
                 __builtin_amdgcn_exp2f(l3 * CE - mc2)};
      u32x2 eb = pk4u(e);
      f32x4 z = {};
      f32x4 sum = mma16(ones, __builtin_bit_cast(bf16x4, eb), z);
      f32x4 o = mma16(vf[b], __builtin_bit_cast(bf16x4, eb & mp), z);
      o = mma16(vf[b + 1], __builtin_bit_cast(bf16x4, eb & mc), o);
      float inv = __builtin_amdgcn_rcpf(sum[0]);
      ob[b] = pk4u(o * inv);
    }
    rsrc_t rO = mk(ws + WS_O, 2u << 20);
    const unsigned vo = (((p_c * 4 + wid) * 2 + (g >> 1)) * 2048 + p_r * 128 + (g & 1) * 16 + i) * 16;
#pragma unroll
    for (int P = 0; P < 4; ++P) __builtin_amdgcn_raw_buffer_store_b128(swap16(ob[2 * P], ob[2 * P + 1]), rO, vo + P * 512, 0, SC1);
    STAMP(2);
    publish(fl + FL_O + (p_r * 8 + p_c) * 4 + wid, tag, lane);
    STAMP(3);
  }
  f32x4 keep[4];
  {
    unsigned sb[2];
#pragma unroll
    for (int ks = 0; ks < 2; ++ks) sb[ks] = (wc * 64 + i) * 128 + (((4 * ks + g) ^ (i >> 1)) * 16);
    const unsigned ab = 16384 + g * 2048 + (wr * 64 + i) * 16;
    f32x4 acc[4][4] = {};
    bf16x8 fa[2][4], fb[2][4];
    auto rd = [&](int ks, int slot) {
      const char* l = shm + slot * SLOT;
#pragma unroll
      for (int m = 0; m < 4; ++m) fa[ks][m] = lds128(l + ab + ks * 8192 + m * 256);
#pragma unroll
      for (int n = 0; n < 4; ++n) fb[ks][n] = lds128(l + sb[ks] + n * 2048);
    };
    auto mm = [&](int ks) {
#pragma unroll
      for (int m = 0; m < 4; ++m)
#pragma unroll
        for (int n = 0; n < 4; ++n) acc[m][n] = mma(fa[ks][m], fb[ks][n], acc[m][n]);
    };
    auto sched = [&]() {
#pragma unroll
      for (int q = 0; q < 8; ++q) { SGB(0x8, 1); SGB(0x100, 1); }
      SGB(0x8, 8);
    };
    int slot = 0;
    BAR();
    STAMP(4);
    rd(0, slot); SCHED0(); rd(1, slot); mm(0); sched(); WAIT_L0(); SCHED0();
#pragma unroll 1
    for (int k = 1; k < 16; ++k) {
      slot = slot == 4 ? 0 : slot + 1;
      BAR(); rd(0, slot); mm(1); sched(); SCHED0(); rd(1, slot); mm(0); sched(); WAIT_L0(); SCHED0();
    }
    mm(1);
    STAMP(5);
    rsrc_t rP = mk(ws + WS_PART, 8u << 20);
    const unsigned pb = (b_mt * 4 + b_nt) * 131072 + b_kq * 8192 + wid * 2048;
#pragma unroll
    for (int n = 0; n < 4; ++n) {
      if (n != b_kq) {
#pragma unroll
        for (int P = 0; P < 2; ++P)
          __builtin_amdgcn_raw_buffer_store_b128(pk8(acc[2 * P][n], acc[2 * P + 1][n]), rP, pb + n * 32768 + lane * 16 + P * 1024, 0, SC1);
      } else {
#pragma unroll
        for (int m = 0; m < 4; ++m) keep[m] = acc[m][n];
      }
    }
    STAMP(6);
  }
  {
    const int q4 = i >> 2, p4 = i & 3;
    const int sw = 2 * (((g & 1) << 1) | (q4 >> 1));
    unsigned wa[2];
#pragma unroll
    for (int a = 0; a < 2; ++a) wa[a] = (8 * g + q4) * 128 + (((wr * 4 + 2 * a + (p4 >> 1)) ^ sw) * 16) + 8 * (p4 & 1);
    const unsigned oa = 16384 + g * 1024 + (wc * 32 + i) * 16;
    f32x4 acc[2][2] = {};
    bf16x8 fa[4][2], fb[4][2];
    auto rd = [&](int slot) {
      const char* l = shm + slot * SLOT;
#pragma unroll
      for (int ks = 0; ks < 4; ++ks) {
#pragma unroll
        for (int a = 0; a < 2; ++a) fa[ks][a] = cat(ldstr(l + wa[a] + ks * 4096), ldstr(l + wa[a] + ks * 4096 + 512));
#pragma unroll
        for (int b = 0; b < 2; ++b) fb[ks][b] = lds128(l + oa + ks * 4096 + b * 256);
      }
    };
    auto mm = [&]() {
#pragma unroll
      for (int ks = 0; ks < 4; ++ks)
#pragma unroll
        for (int a = 0; a < 2; ++a)
#pragma unroll
          for (int b = 0; b < 2; ++b) acc[a][b] = mma(fa[ks][a], fb[ks][b], acc[a][b]);
    };
    BAR(); rd(1); mm(); SCHED0();
    publish(fl + FL_P + ((b_mt * 4 + b_nt) * 4 + wid) * 4 + b_kq, tag, lane);
    STAMP(7);
#pragma unroll 1
    for (int k = 1; k < 4; ++k) { BAR(); rd(1 + k); mm(); SCHED0(); }
    STAMP(8);
    rsrc_t rOut = mk(out, 4u << 20);
#pragma unroll
    for (int b = 0; b < 2; ++b) {
      u32x4 v = swap16(pk4u(acc[0][b]), pk4u(acc[1][b]));
      int pos = o_pb * 64 + wc * 32 + b * 16 + i, t = 2 * (pos & 1023) + (pos >> 10);
      int n = o_nt * 64 + wr * 32 + 16 * (g & 1) + 8 * (g >> 1);
      __builtin_amdgcn_raw_buffer_store_b128(v, rOut, t * 2048 + 1024 + n * 2, 0, 0);
    }
  }
  {
    wait_flags(fl + FL_P + ((b_mt * 4 + b_nt) * 4 + wid) * 4 + (lane & 3), tag);
    STAMP(9);
    rsrc_t rP = mk(ws + WS_PART, 8u << 20);
    const unsigned pb = (b_mt * 4 + b_nt) * 131072 + b_kq * 32768 + wid * 2048;
    u32x4 v[3][2];
#pragma unroll
    for (int s = 0; s < 3; ++s)
#pragma unroll
      for (int P = 0; P < 2; ++P)
        v[s][P] = __builtin_amdgcn_raw_buffer_load_b128(rP, lane * 16 + P * 1024, pb + ((b_kq + 1 + s) & 3) * 8192, SC1);
    rsrc_t rOut = mk(out, 4u << 20);
    int t = b_mt * 128 + wc * 64 + b_kq * 16 + i;
#pragma unroll
    for (int P = 0; P < 2; ++P) {
      f32x4 lo = keep[2 * P], hi = keep[2 * P + 1];
#pragma unroll
      for (int s = 0; s < 3; ++s) {
        f32x4 a = {blo(v[s][P][0]), bhi(v[s][P][0]), blo(v[s][P][1]), bhi(v[s][P][1])};
        f32x4 b = {blo(v[s][P][2]), bhi(v[s][P][2]), blo(v[s][P][3]), bhi(v[s][P][3])};
        lo += a; hi += b;
      }
      int e = b_nt * 128 + wr * 64 + 32 * P + 8 * g;
      __builtin_amdgcn_raw_buffer_store_b128(pk8(lo, hi), rOut, t * 2048 + e * 2, 0, 0);
    }
    STAMP(10);
  }
}

__global__ void tiny(char* ws) { if (threadIdx.x == 0 && blockIdx.x == 0) *(u64*)(ws + (200u << 20)) = wall_clock64(); }

extern "C" void kernel_launch(void* const* d_in, const int* in_sizes, int n_in, void* d_out, int out_size, void* d_ws, size_t ws_size,
                              hipStream_t stream) {
  (void)hipFuncSetAttribute((const void*)fused, hipFuncAttributeMaxDynamicSharedMemorySize, LDS_BYTES);
  tiny<<<256, 64, 0, stream>>>((char*)d_ws);
  fused<<<256, 512, LDS_BYTES, stream>>>((const char*)d_in[0], (const char*)d_in[1], (const char*)d_in[2], (const char*)d_in[3],
                                         (const char*)d_in[4], (const char*)d_in[5], (const char*)d_in[6], (const char*)d_in[7],
                                         (char*)d_out, (char*)d_ws);
}
```

```cpp
#include <hip/hip_runtime.h>
#include <hip/hip_bf16.h>

typedef __attribute__((ext_vector_type(8))) short bf16x8;
typedef __attribute__((ext_vector_type(4))) short bf16x4;
typedef __attribute__((ext_vector_type(4))) float f32x4;
typedef __attribute__((ext_vector_type(2))) float f32x2;
typedef __attribute__((ext_vector_type(2))) __bf16 bf2;
typedef __attribute__((ext_vector_type(4))) unsigned u32x4;
typedef __attribute__((ext_vector_type(2))) unsigned u32x2;
typedef unsigned long long u64;
typedef __amdgpu_buffer_rsrc_t rsrc_t;
#define LAS __attribute__((address_space(3)))
#define GAS __attribute__((address_space(1)))
#define DEV __device__ __forceinline__

#define WAIT_V(n) asm volatile("s_waitcnt vmcnt(%0)" ::"n"(n) : "memory")
#define WAIT_L0() asm volatile("s_waitcnt lgkmcnt(0)" ::: "memory")
#define BAR() do { asm volatile("" ::: "memory"); __builtin_amdgcn_s_barrier(); asm volatile("" ::: "memory"); } while (0)
#define SCHED0() __builtin_amdgcn_sched_barrier(0)
#define SGB(m, n) __builtin_amdgcn_sched_group_barrier(m, n, 0)

constexpr int SLOT = 32768, LDS_BYTES = 163840;
constexpr int QSLOT = 43008, QBASE = LDS_BYTES - 3 * QSLOT, QW = 18432;
constexpr unsigned WS_AB = 0, WS_O = 4u << 20, WS_FLAG = 6u << 20, WS_PART = 8u << 20, WS_BYTES = 16u << 20;
constexpr unsigned FL_AB = 128, FL_O = 1024, FL_P = 4096;
constexpr int SC1 = 16;
constexpr int SPIN_CAP = 1 << 16;

DEV void dma16(rsrc_t r, unsigned voff, unsigned soff, char* lds) {
  __builtin_amdgcn_raw_ptr_buffer_load_lds(r, (LAS void*)lds, 16, voff, soff, 0, SC1);
}
DEV rsrc_t mk(const void* p, unsigned bytes) { return __builtin_amdgcn_make_buffer_rsrc((void*)p, 0, bytes, 0x00020000); }
DEV void st16(u32x4 v, rsrc_t r, unsigned off) { __builtin_amdgcn_raw_buffer_store_b128(v, r, off, 0, SC1); }
DEV unsigned pk(float a, float b) { f32x2 v = {a, b}; return __builtin_bit_cast(unsigned, __builtin_convertvector(v, bf2)); }
DEV u32x2 pk4u(f32x4 v) { u32x2 r = {pk(v[0], v[1]), pk(v[2], v[3])}; return r; }
DEV bf16x4 pk4(f32x4 v) { return __builtin_bit_cast(bf16x4, pk4u(v)); }
DEV u32x4 pk8(f32x4 lo, f32x4 hi) { u32x4 v = {pk(lo[0], lo[1]), pk(lo[2], lo[3]), pk(hi[0], hi[1]), pk(hi[2], hi[3])}; return v; }
DEV float blo(unsigned u) { return __builtin_bit_cast(float, u << 16); }
DEV float bhi(unsigned u) { return __builtin_bit_cast(float, u & 0xffff0000u); }
DEV bf16x8 lds128(const char* p) { return *(const bf16x8*)p; }
DEV bf16x4 ldstr(const char* p) { return __builtin_amdgcn_ds_read_tr16_b64_v4i16((LAS bf16x4*)p); }
DEV bf16x8 cat(bf16x4 a, bf16x4 b) { return __builtin_shufflevector(a, b, 0, 1, 2, 3, 4, 5, 6, 7); }
DEV f32x4 mma(bf16x8 a, bf16x8 b, f32x4 c) { return __builtin_amdgcn_mfma_f32_16x16x32_bf16(a, b, c, 0, 0, 0); }
DEV f32x4 mma16(bf16x4 a, bf16x4 b, f32x4 c) { return __builtin_amdgcn_mfma_f32_16x16x16bf16_1k(a, b, c, 0, 0, 0); }
DEV float vmax(float a, float b) { return __builtin_elementwise_maximum(a, b); }
DEV unsigned sel(unsigned m, unsigned a, unsigned b) { return __builtin_amdgcn_bitop3_b32(m, a, b, 0xca); }
DEV float cpyf(float x) { asm("" : "+v"(x)); return x; }
DEV void sw32(float& a, float& b) {
  auto r = __builtin_amdgcn_permlane32_swap(__builtin_bit_cast(unsigned, a), __builtin_bit_cast(unsigned, b), false, false);
  unsigned r0 = r[0], r1 = r[1];
  a = __builtin_bit_cast(float, r0); b = __builtin_bit_cast(float, r1);
}
DEV void sw16(float& a, float& b) {
  auto r = __builtin_amdgcn_permlane16_swap(__builtin_bit_cast(unsigned, a), __builtin_bit_cast(unsigned, b), false, false);
  unsigned r0 = r[0], r1 = r[1];
  a = __builtin_bit_cast(float, r0); b = __builtin_bit_cast(float, r1);
}
DEV void red4max4(float (&m)[4]) {
  sw32(m[0], m[1]); sw32(m[2], m[3]);
  float x = vmax(m[0], m[1]), y = vmax(m[2], m[3]);
  sw16(x, y);
  float z = vmax(x, y);
  float z2 = cpyf(z);
  sw16(z, z2);
  m[0] = z; m[1] = cpyf(z); m[2] = z2; m[3] = cpyf(z2);
  sw32(m[0], m[1]); sw32(m[2], m[3]);
}
DEV u32x4 swap16(u32x2 x, u32x2 y) {
  auto a = __builtin_amdgcn_permlane16_swap(x[0], y[0], false, false);
  auto b = __builtin_amdgcn_permlane16_swap(x[1], y[1], false, false);
  unsigned a0 = a[0], a1 = a[1], b0 = b[0], b1 = b[1];
  u32x4 r = {a0, b0, a1, b1};
  return r;
}
DEV u64 ld_agent(const u64* p) { return __hip_atomic_load((GAS const u64*)p, __ATOMIC_RELAXED, __HIP_MEMORY_SCOPE_AGENT); }
DEV void st_agent(u64* p, u64 v) { __hip_atomic_store((GAS u64*)p, v, __ATOMIC_RELAXED, __HIP_MEMORY_SCOPE_AGENT); }
#define LD_ASYNC(R, p) asm volatile("global_load_dwordx2 " R ", %0, off sc1" ::"v"(p) : "memory", "v250", "v251", "v252", "v253", "v254", "v255")
#define LD_A(p) LD_ASYNC("v[250:251]", p)
#define LD_B(p) LD_ASYNC("v[252:253]", p)
#define LD_C(p) LD_ASYNC("v[254:255]", p)
#define LANDED(R) ({ u64 v_; asm volatile("v_mov_b64 %0, " R : "=v"(v_)::"memory"); v_; })
#define LANDED_A() LANDED("v[250:251]")
#define LANDED_B() LANDED("v[252:253]")
#define LANDED_C() LANDED("v[254:255]")
DEV void touch(const void* p) { asm volatile("global_load_dword v249, %0, off sc1" ::"v"(p) : "memory", "v249"); }
DEV u64 mk_tag(u64 e) {
  unsigned lo = __builtin_amdgcn_readfirstlane((unsigned)e), hi = __builtin_amdgcn_readfirstlane((unsigned)(e >> 32));
  u64 x = ((u64)hi << 32) | lo;
  x = (x ^ (x >> 31)) * 0x9E3779B97F4A7C15ull + 0xD1B54A32D192ED03ull;
  return x ^ (x >> 29);
}
DEV u64 wait_flags(const u64* p, u64 tag, u64 need) {
  u64 m = 0;
#pragma unroll 1
  for (int spins = 0; spins < SPIN_CAP; ++spins) {
    m = __ballot(ld_agent(p) == tag);
    if ((m & need) == need) break;
    __builtin_amdgcn_s_sleep(4);
  }
  asm volatile("" ::: "memory");
  return m | need;
}

__global__ __launch_bounds__(512, 2) void fused(const char* __restrict__ xA, const char* __restrict__ Sr, const char* __restrict__ Si,
                                                const char* __restrict__ Dr, const char* __restrict__ Di, const char* __restrict__ xB,
                                                const char* __restrict__ wqkv, const char* __restrict__ wo, char* __restrict__ out,
                                                char* __restrict__ ws) {
  extern __shared__ __attribute__((aligned(16))) char shm[];
  const char* pc0;
  unsigned code_bytes;
  asm volatile("s_getpc_b64 %0\n.Lfz_pc:\n\ts_mov_b32 %1, .Lfz_end-.Lfz_pc" : "=s"(pc0), "=s"(code_bytes));
  const int bid = blockIdx.x, xcd = bid & 7, loc = bid >> 3;
  const int wid = __builtin_amdgcn_readfirstlane(threadIdx.x >> 6), lane = threadIdx.x & 63;
  asm volatile("s_cmp_lg_u32 %0, 0\n\ts_cbranch_scc1 .Lfz_skip\n\tv_lshl_add_u32 v249, %1, 7, %2\n\tv_min_u32 v249, %3, v249\n\t"
               "global_load_dword v249, v249, %4\n.Lfz_skip:" ::"s"(wid), "v"(lane), "s"((loc & 3) * 8192), "s"(code_bytes), "s"(pc0)
               : "memory", "v249", "scc");
  const bool isq = loc >= 16;
  const int l16 = loc & 15;
  const int p_r = 4 * (xcd & 3) + (l16 & 3), p_c = 4 * (xcd >> 2) + (l16 >> 2);
  const int b_kq = ((xcd >> 1) + 2) & 3, b_mh = xcd & 1, b_mt = 8 * b_mh + (loc >> 2), b_nt = loc & 3;
  const int o_pb = 8 * (xcd & 3) + (loc & 7), o_nt = 4 * (xcd >> 2) + (loc >> 3);

  if (__builtin_expect(wid >= 4, 1)) {
    __builtin_amdgcn_s_setprio(3);
    const int lw = wid & 1, role = (wid >> 1) & 1;
    const int r = lane >> 3, p = lane & 7;
    const int rh = ((l16 & 3) + (l16 >> 2)) & 3;
    const u64 o01 = isq ? 0x7643205176532140ull : 0x7653241075436120ull, o23 = isq ? 0x6542170375431602ull : 0x7643250165427031ull;
    const unsigned ord1 = (unsigned)(((rh & 2) ? o23 : o01) >> ((rh & 1) * 32));
    const bool qw = isq && role, qx = isq && !role;
    const unsigned RS = 1024 * (1 + (int)isq + (int)qw);
    const unsigned bw = lw * 32 * 3072 + p_c * 128, bx = (((p_r & 7) * 128 + lw * 64) * 2 + (p_r >> 3)) * 1024;
    const unsigned ba = ((role ? (p_c & 3) : p_r) * 128 + lw * 64) * 1024;
    const unsigned bq = role ? bw : bx, b0 = isq ? bq : ba;
    const unsigned GB = qw ? 1024 : 32 * RS, GC = qw ? 8192 : 4096;
    const unsigned kstep = qw ? 64 * 3072 : 128;
    const unsigned lq = role ? QBASE + QW : QBASE + 2048, la = role * 16384;
    const unsigned l0 = (isq ? lq : la) + lw * (qw ? 4096 : 8192);
    const unsigned slsz = isq ? QSLOT : SLOT, nsl = isq ? 3 : 5;
    const unsigned fr = (r >> (qw ? 0 : 1)) & (qw ? 2 : qx ? 3 : 1);
    unsigned vo[8];
#pragma unroll
    for (int j = 0; j < 4; ++j) vo[j] = b0 + (r + 8 * j) * RS + ((p ^ fr ^ (isq ? (j & 1) << 2 : j << 1)) << 4);
    const unsigned vh = (((p_r & 7) * 128 + ((p_r & 7) ? -16 : 0) + lw * 8 + r) * 2 + (p_r >> 3)) * 1024 + ((p ^ fr ^ (lw << 2)) << 4);
    int late = role;
    asm volatile("" ::"v"(vo[0]), "v"(vo[1]), "v"(vo[2]), "v"(vo[3]));
    asm volatile("" : "+s"(late));
    const char* pD = (p_c >> 2) + late == 2 ? Di : Dr;
    const char* pq = late ? wqkv : xB;
    const char* pa = late ? pD : xA;
    rsrc_t rs = mk(isq ? pq : pa, qw ? 1536u << 10 : (role ? 512u << 10 : 2u << 20));
    auto issue1 = [&](int k, unsigned sl) {
      const unsigned so = ((ord1 >> (4 * k)) & 7) * kstep, lo = l0 + sl * slsz;
#pragma unroll
      for (int j = 0; j < 4; ++j) dma16(rs, vo[j], so, shm + lo + j * 1024);
#pragma unroll
      for (int j = 0; j < 4; ++j) dma16(rs, vo[j], so + GB, shm + lo + GC + j * 1024);
      if (qw) {
#pragma unroll
        for (int j = 0; j < 4; ++j) dma16(rs, vo[j], so + 2 * GB, shm + lo + 2 * GC + j * 1024);
      } else if (qx) dma16(rs, vh, so, shm + QBASE + sl * QSLOT + lw * 1024);
    };
    unsigned sl = isq ? 1 : 2;
    issue1(0, sl); issue1(1, sl + 1);
    sl = isq ? 0 : 4;
    char* wsl = ws;
    asm volatile("" : "+s"(wsl));
    u64* const fl = (u64*)(wsl + WS_FLAG);
#pragma unroll 1
    for (int g = 0; g < 6; ++g) {
      if (qw) WAIT_V(12); else if (qx) WAIT_V(9); else WAIT_V(8);
      BAR();
      if (g == 0) LD_A(fl);
      issue1(g + 2, sl); sl = sl + 1 == nsl ? 0 : sl + 1;
    }
    auto issue = [&](unsigned so, unsigned lo) {
#pragma unroll
      for (int i = 0; i < 8; ++i) dma16(rs, vo[i], so, shm + lo + i * 1024);
    };
    const unsigned lhalf = role * 16384 + lw * 8192;
    const u64 tag = mk_tag(LANDED_A());
    const int rho = (b_nt + (loc >> 2)) & 3;
    const u64 oa = (rho & 1) ? 0xfecbad8769432501ull : 0xfedba9c765832140ull, ob = (rho & 1) ? 0xedcfa98b65472103ull : 0xfdcbe987a5436102ull;
    const u64 ord = (rho & 2) ? ob : oa;
    auto ko = [&](int t) { return (unsigned)(ord >> (4 * t)) & 15u; };
    unsigned step;
    if (role) {
      const u64* fab = fl + FL_AB + (((b_kq >> 1) * 4 + b_nt) * 16 + (b_kq & 1) * 8) * 4 + (lane & 31);
      const u64* fo = fl + FL_O + (o_pb >> 1) * 32 + (lane & 31);
      if (qw) WAIT_V(12); else WAIT_V(8);
      BAR(); WAIT_V(0); BAR(); BAR();
      rs = mk(wsl, WS_BYTES);
      const unsigned v = WS_AB + ((b_kq >> 1) * 4 + b_nt) * 524288 + (b_kq & 1) * 262144 + lw * 8192 + lane * 16;
#pragma unroll
      for (int i = 0; i < 8; ++i) vo[i] = v + i * 1024;
      step = 16384;
      if (!isq) __builtin_amdgcn_s_sleep(12);
      u64 ready = wait_flags(fab, tag, (15ull << (4 * (ko(0) >> 1))) | (15ull << (4 * (ko(1) >> 1))));
      if (ready != ~0ull) LD_B(fab);
      issue(ko(0) * step, lhalf); issue(ko(1) * step, SLOT + lhalf);
      sl = 2;
      auto next = [&](int k) {
        const int kk = ko(k);
        if (ready != ~0ull) {
          const u64 need = 15ull << (4 * (kk >> 1));
          if ((ready & need) != need) ready |= wait_flags(fab, tag, need);
        }
        issue(kk * step, sl * SLOT + lhalf); sl = sl == 4 ? 0 : sl + 1;
      };
      WAIT_V(8); BAR();
      if (ready != ~0ull) ready |= __ballot(LANDED_B() == tag);
      next(2);
      if (ready != ~0ull) LD_B(fab);
      next(3);
#pragma unroll 1
      for (int k = 4; k < 16; ++k) {
        WAIT_V(16); BAR();
        if (ready != ~0ull && (k & 1)) { ready |= __ballot(LANDED_B() == tag); if (ready != ~0ull) LD_B(fab); }
        if (k == 13) LD_C(fo);
        next(k);
      }
      WAIT_V(16); BAR();
#pragma unroll
      for (int i = 0; i < 8; ++i) vo[i] = WS_O + o_pb * 65536 + lw * 8192 + i * 1024 + lane * 16;
      if (!__all(LANDED_C() == tag)) wait_flags(fo, tag, ~0ull);
    } else {
      rs = mk((b_kq >> 1) ? Si : Sr, 8u << 20);
      const unsigned v = (b_mt * 128 + lw * 64 + r) * 4096 + (b_kq & 1) * 2048 + ((p ^ (r >> 1)) * 16);
#pragma unroll
      for (int i = 0; i < 8; ++i) vo[i] = (v ^ ((i & 1) << 6)) + ((i + 2 * b_kq) & 7) * 32768;
      step = 128;
      if (isq) {
        issue(ko(0) * step, lhalf);
        WAIT_V(17); BAR();
        issue(ko(1) * step, SLOT + lhalf);
        WAIT_V(16); BAR(); BAR();
      } else {
        WAIT_V(8); BAR(); WAIT_V(0); BAR(); BAR();
        issue(ko(0) * step, lhalf); issue(ko(1) * step, SLOT + lhalf);
      }
      WAIT_V(8); BAR();
      issue(ko(2) * step, 2 * SLOT + lhalf); issue(ko(3) * step, 3 * SLOT + lhalf);
      WAIT_V(16); BAR();
      issue(ko(4) * step, 4 * SLOT + lhalf);
      if (!isq) touch(wo + ((l16 * 2 + lw) * 16 + (lane >> 2)) * 1024 + (xcd >> 2) * 512 + (lane & 3) * 128);
      sl = 0;
#pragma unroll 1
      for (int k = 5; k < 16; ++k) {
        WAIT_V(16); BAR();
        issue(ko(k) * step, sl * SLOT + lhalf); sl = sl == 4 ? 0 : sl + 1;
      }
      WAIT_V(16); BAR();
      rs = mk(wo, 512u << 10);
      const unsigned w = (lw * 64 + r) * 1024 + o_nt * 128 + ((p ^ (r & 2)) * 16);
#pragma unroll
      for (int i = 0; i < 8; ++i) vo[i] = (w ^ ((i & 1) << 6)) + i * 8192;
      step = 131072;
    }
    issue(0, 1 * SLOT + lhalf);
    WAIT_V(16); BAR(); issue(step, 2 * SLOT + lhalf);
    WAIT_V(16); BAR(); issue(2 * step, 3 * SLOT + lhalf);
    WAIT_V(16); BAR(); issue(3 * step, 4 * SLOT + lhalf);
    WAIT_V(16); BAR(); WAIT_V(8); BAR(); WAIT_V(0); BAR();
    if (bid == 0 && wid == 6) {
#pragma unroll 1
      for (int spins = 0; spins < SPIN_CAP; ++spins) {
        bool ok = true;
#pragma unroll
        for (int j = 0; j < 8; ++j) ok &= (ld_agent(fl + FL_AB + j * 64 + lane) == tag) & (ld_agent(fl + FL_O + j * 64 + lane) == tag);
        if (__all(ok)) break;
        __builtin_amdgcn_s_sleep(2);
      }
      if (lane == 0) st_agent(fl, tag);
    }
    return;
  }

  const int g = lane >> 4, i = lane & 15;
  const int wr = wid >> 1, wc = wid & 1;
  u64* const fl = (u64*)(ws + WS_FLAG);
  const rsrc_t rW = mk(ws, WS_BYTES);
  const unsigned ptile = WS_PART + (b_mt * 4 + b_nt) * 131072 + wid * 2048 + lane * 16;
  u64 tag;
  if (wid == 2) {
    const char* S = ((b_kq >> 1) ? Si : Sr) + b_mh * (4u << 20);
    const char* q0 = lane < 8 ? ws + (lane == 3 ? 1u << 20 : 0u) : S;
    const char* q1 = lane < 14 ? (const char*)out : wo;
    const unsigned idx = lane < 8 ? lane : lane < 11 ? lane - 8 : lane < 14 ? lane - 11 : 0;
    const char* q = (lane < 11 ? q0 : q1) + idx * ((2u << 20) - (lane < 8 ? 0 : 64));
    if (lane < 15) touch(q);
  }
  LD_A(fl);
  BAR();
  auto sched16 = [&]() {
#pragma unroll
    for (int q = 0; q < 8; ++q) { SGB(0x8, 1); SGB(0x100, 1); SGB(0x8, 1); }
  };
  if (!isq) {
    const int f = ((i >> 2) << 1) | ((i >> 1) & 1), rin = 8 * (i >> 2) + (i & 3);
    const char* aa[2][3];
#pragma unroll
    for (int h = 0; h < 3; ++h)
#pragma unroll
      for (int ks = 0; ks < 2; ++ks) aa[ks][h] = shm + h * 2 * SLOT + rin * 128 + (((4 * ks + g) ^ f) * 16);
    const unsigned ao = wr * 8192, bo = wc * 8192;
    f32x4 acc[4][4] = {};
    bf16x8 fa[2][4], fb[2][4];
    auto rd = [&](int ks, int slot) {
      const char* pa = aa[ks][slot >> 1] + ao + (slot & 1) * SLOT;
      const char* pb = aa[ks][slot >> 1] + bo + (slot & 1) * SLOT + 16384;
      fa[ks][0] = lds128(pa);
#pragma unroll
      for (int n = 0; n < 4; ++n) fb[ks][n] = lds128(pb + (n >> 1) * 4096 + (n & 1) * 512);
#pragma unroll
      for (int m = 1; m < 4; ++m) fa[ks][m] = lds128(pa + (m >> 1) * 4096 + (m & 1) * 512);
    };
    auto mm = [&](int ks) {
#pragma unroll
      for (int m = 0; m < 4; ++m)
#pragma unroll
        for (int n = 0; n < 4; ++n) acc[m][n] = mma(fa[ks][m], fb[ks][n], acc[m][n]);
    };
    rd(0, 2); SCHED0(); rd(1, 2); mm(0); sched16(); SCHED0();
#pragma unroll
    for (int k = 1; k < 8; ++k) {
      BAR(); rd(0, (k + 2) % 5); mm(1); sched16(); SCHED0(); rd(1, (k + 2) % 5); mm(0); sched16(); SCHED0();
    }
    WAIT_V(0);
    tag = mk_tag(LANDED_A());
    mm(1);
    const unsigned vo = WS_AB + p_c * 524288 + (p_r * 16 + wr * 8 + g) * 2048 + (wc * 64 + i) * 16;
#pragma unroll
    for (int P = 0; P < 2; ++P)
#pragma unroll
      for (int n = 0; n < 4; ++n) st16(pk8(acc[2 * P][n], acc[2 * P + 1][n]), rW, vo + P * 8192 + n * 256);
    WAIT_V(0);
    BAR();
    if (wid == 0 && lane < 4) st_agent(fl + FL_AB + (p_c * 16 + p_r) * 4 + lane, tag);
  } else {
    const int q4 = i >> 2, p4 = i & 3;
    const char* xa[2][3];
    const char* wa[3];
#pragma unroll
    for (int h = 0; h < 3; ++h) {
#pragma unroll
      for (int ks = 0; ks < 2; ++ks) xa[ks][h] = shm + QBASE + h * QSLOT + i * 128 + (((4 * ks + g) ^ (i >> 1)) * 16);
      wa[h] = shm + QBASE + h * QSLOT + QW + (8 * g + q4) * 128 + (((2 * wid + (p4 >> 1)) ^ (2 * (((g & 1) << 1) | (q4 >> 1)))) * 16) + 8 * (p4 & 1);
    }
    f32x4 aq[8] = {}, ak[9] = {}, av[9] = {};
    bf16x8 fx[2][9], fw[2][3];
    auto rd = [&](int ks, int slot) {
      fw[ks][1] = cat(ldstr(wa[slot] + ks * 4096 + 8192), ldstr(wa[slot] + ks * 4096 + 512 + 8192));
      fx[ks][0] = lds128(xa[ks][slot]);
      fw[ks][2] = cat(ldstr(wa[slot] + ks * 4096 + 16384), ldstr(wa[slot] + ks * 4096 + 512 + 16384));
      fw[ks][0] = cat(ldstr(wa[slot] + ks * 4096), ldstr(wa[slot] + ks * 4096 + 512));
#pragma unroll
      for (int b = 1; b < 9; ++b) fx[ks][b] = lds128(xa[ks][slot] + b * 2048);
    };
    auto mm = [&](int ks) {
#pragma unroll
      for (int b = 0; b < 9; ++b) {
        if (b) aq[b - 1] = mma(fw[ks][0], fx[ks][b], aq[b - 1]);
        ak[b] = mma(fw[ks][1], fx[ks][b], ak[b]);
        av[b] = mma(fx[ks][b], fw[ks][2], av[b]);
      }
    };
    auto sched0 = [&]() {
#pragma unroll
      for (int q = 0; q < 4; ++q) { SGB(0x8, 1); SGB(0x100, 1); }
#pragma unroll
      for (int q = 0; q < 11; ++q) { SGB(0x8, 1); SGB(0x100, 1); SGB(0x8, 1); }
    };
    auto sched1 = [&]() {
#pragma unroll
      for (int q = 0; q < 5; ++q) { SGB(0x8, 1); SGB(0x100, 1); SGB(0x8, 1); SGB(0x100, 1); SGB(0x8, 2); SGB(0x100, 1); }
      SGB(0x8, 6);
    };
#define QSTAGE(slot) do { BAR(); rd(0, slot); mm(1); sched0(); SCHED0(); rd(1, slot); mm(0); sched1(); WAIT_L0(); SCHED0(); } while (0)
    rd(0, 1); SCHED0(); rd(1, 1); mm(0); sched1(); WAIT_L0(); SCHED0();
#pragma unroll 1
    for (int ko = 0; ko < 2; ++ko) {
      QSTAGE(2); QSTAGE(0); QSTAGE(1);
    }
    QSTAGE(2);
    BAR();
    WAIT_V(0);
    tag = mk_tag(LANDED_A());
    mm(1);
    bf16x4 kf[9], vf[9], qf[8];
#pragma unroll
    for (int b = 0; b < 9; ++b) { kf[b] = pk4(ak[b]); vf[b] = pk4(av[b]); }
#pragma unroll
    for (int b = 0; b < 8; ++b) qf[b] = pk4(aq[b]);
    constexpr float NEG = -3.0e38f;
    f32x4 cp, cc, c0;
    u32x2 mc;
#pragma unroll
    for (int r = 0; r < 4; ++r) {
      int key = 4 * g + r;
      cp[r] = key >= i + 9 ? 0.f : NEG; cc[r] = key >= i - 7 ? 0.f : NEG;
      c0[r] = ((p_r & 7) == 0) ? NEG : cp[r];
      if (!(r & 1)) mc[r >> 1] = key <= i ? 0xffffu : 0u;
      else mc[r >> 1] |= key <= i ? 0xffff0000u : 0u;
    }
    f32x4 sp[8], sc[8];
    u32x2 lg[8];
#pragma unroll
    for (int b = 0; b < 8; ++b) { sp[b] = mma16(kf[b], qf[b], b == 0 ? c0 : cp); sc[b] = mma16(kf[b + 1], qf[b], cc); }
#pragma unroll
    for (int b = 0; b < 8; ++b) {
      u32x2 x = pk4u(sp[b]), y = pk4u(sc[b]);
      lg[b][0] = sel(mc[0], y[0], x[0]); lg[b][1] = sel(mc[1], y[1], x[1]);
    }
    SCHED0();
    const unsigned ovo = WS_O + p_r * 131072 + ((p_c * 4 + wid) * 2 + (g >> 1)) * 1024 + ((g & 1) * 16 + i) * 16;
    constexpr float CE = 0.25f * 1.4426950408889634f;
    const bf16x4 ones = {0x3f80, 0x3f80, 0x3f80, 0x3f80};
#pragma unroll
    for (int h = 0; h < 2; ++h) {
      float mx[4];
      u32x2 eb[4], ob[4];
      f32x4 z = {}, sum[4], ov[4];
#pragma unroll
      for (int b = 0; b < 4; ++b) { u32x2 l = lg[h * 4 + b]; mx[b] = vmax(vmax(vmax(blo(l[0]), bhi(l[0])), blo(l[1])), bhi(l[1])); }
      red4max4(mx);
#pragma unroll
      for (int b = 0; b < 4; ++b) {
        u32x2 l = lg[h * 4 + b];
        float m2 = mx[b] * CE;
        f32x4 e = {__builtin_amdgcn_exp2f(blo(l[0]) * CE - m2), __builtin_amdgcn_exp2f(bhi(l[0]) * CE - m2),
                   __builtin_amdgcn_exp2f(blo(l[1]) * CE - m2), __builtin_amdgcn_exp2f(bhi(l[1]) * CE - m2)};
        eb[b] = pk4u(e);
      }
      SCHED0();
#pragma unroll
      for (int b = 0; b < 4; ++b) {
        sum[b] = mma16(ones, __builtin_bit_cast(bf16x4, eb[b]), z);
        ov[b] = mma16(vf[h * 4 + b], __builtin_bit_cast(bf16x4, eb[b] & ~mc), z);
      }
#pragma unroll
      for (int b = 0; b < 4; ++b) ov[b] = mma16(vf[h * 4 + b + 1], __builtin_bit_cast(bf16x4, eb[b] & mc), ov[b]);
      SCHED0();
#pragma unroll
      for (int b = 0; b < 4; ++b) ob[b] = pk4u(ov[b] * __builtin_amdgcn_rcpf(sum[b][0]));
#pragma unroll
      for (int P = 0; P < 2; ++P) st16(swap16(ob[2 * P], ob[2 * P + 1]), rW, ovo + h * 65536 + P * 512);
      SCHED0();
    }
  }
  f32x4 keep[4];
  {
    const char* sb[2][3];
    const char* ab[3];
#pragma unroll
    for (int h = 0; h < 3; ++h) {
#pragma unroll
      for (int ks = 0; ks < 2; ++ks) sb[ks][h] = shm + h * 2 * SLOT + (wc * 64 + i) * 128 + (((4 * ks + g) ^ (i >> 1)) * 16);
      ab[h] = shm + h * 2 * SLOT + 16384 + g * 2048 + (wr * 64 + i) * 16;
    }
    f32x4 acc[4][4] = {};
    bf16x8 fa[2][4], fb[2][4];
    auto rd = [&](int ks, int slot) {
      const int h = slot >> 1, o = (slot & 1) * SLOT;
      fa[ks][0] = lds128(ab[h] + o + ks * 8192);
#pragma unroll
      for (int n = 0; n < 4; ++n) fb[ks][n] = lds128(sb[ks][h] + o + n * 2048);
#pragma unroll
      for (int m = 1; m < 4; ++m) fa[ks][m] = lds128(ab[h] + o + ks * 8192 + m * 256);
    };
    auto mm = [&](int ks) {
#pragma unroll
      for (int m = 0; m < 4; ++m)
#pragma unroll
        for (int n = 0; n < 4; ++n) acc[m][n] = mma(fa[ks][m], fb[ks][n], acc[m][n]);
    };
    BAR();
    rd(0, 0); SCHED0(); rd(1, 0); mm(0); sched16(); SCHED0();
#pragma unroll 1
    for (int ko = 0; ko < 3; ++ko) {
#pragma unroll
      for (int k = 1; k <= 5; ++k) { BAR(); rd(0, k % 5); mm(1); sched16(); SCHED0(); rd(1, k % 5); mm(0); sched16(); SCHED0(); }
      if (ko == 0 && isq) {
        WAIT_V(0);
        if (lane == 0) st_agent(fl + FL_O + (p_r * 8 + p_c) * 4 + wid, tag);
      }
    }
    BAR();
    mm(1);
#pragma unroll
    for (int n = 1; n < 4; ++n)
#pragma unroll
      for (int P = 0; P < 2; ++P) st16(pk8(acc[2 * P][n], acc[2 * P + 1][n]), rW, ptile + ((n + b_kq) & 3) * 32768 + b_kq * 8192 + P * 1024);
#pragma unroll
    for (int m = 0; m < 4; ++m) keep[m] = acc[m][0];
    SCHED0();
  }
  const rsrc_t rOut = mk(out, 4u << 20);
  u64* const pfl = fl + FL_P + (b_mt * 4 + b_nt) * 64 + wid * 4;
  const u64* const myp = pfl + b_kq * 16 + ((b_kq + 1 + lane % 3) & 3);
  {
    const int q4 = i >> 2, p4 = i & 3;
    const int sw = 2 * (((g & 1) << 1) | (q4 >> 1));
    unsigned wa[2];
#pragma unroll
    for (int a = 0; a < 2; ++a) wa[a] = (8 * g + q4) * 128 + (((wr * 4 + 2 * a + (p4 >> 1)) ^ sw) * 16) + 8 * (p4 & 1);
    const unsigned oa = 16384 + g * 1024 + (wc * 32 + i) * 16;
    f32x4 acc[2][2] = {};
    bf16x8 fa[2][4][2], fb[2][4][2];
    auto rd = [&](int set, int slot) {
      const char* l = shm + slot * SLOT;
#pragma unroll
      for (int ks = 0; ks < 4; ++ks) {
#pragma unroll
        for (int a = 0; a < 2; ++a) fa[set][ks][a] = cat(ldstr(l + wa[a] + ks * 4096), ldstr(l + wa[a] + ks * 4096 + 512));
#pragma unroll
        for (int b = 0; b < 2; ++b) fb[set][ks][b] = lds128(l + oa + ks * 4096 + b * 256);
      }
    };
    auto mm = [&](int set) {
#pragma unroll
      for (int ks = 0; ks < 4; ++ks)
#pragma unroll
        for (int a = 0; a < 2; ++a)
#pragma unroll
          for (int b = 0; b < 2; ++b) acc[a][b] = mma(fa[set][ks][a], fb[set][ks][b], acc[a][b]);
    };
    rd(0, 1); SCHED0();
    BAR(); rd(1, 2); mm(0); SCHED0();
    WAIT_V(0);
    if (lane < 3) st_agent(pfl + ((b_kq + 1 + lane) & 3) * 16 + b_kq, tag);
    BAR(); rd(0, 3); mm(1); SCHED0();
    BAR(); rd(1, 4); mm(0); SCHED0();
    LD_B(myp);
    mm(1);
#pragma unroll
    for (int b = 0; b < 2; ++b) {
      int pos = o_pb * 64 + wc * 32 + b * 16 + i, t = 2 * (pos & 1023) + (pos >> 10);
      int n = o_nt * 64 + wr * 32 + 16 * (g & 1) + 8 * (g >> 1);
      __builtin_amdgcn_raw_buffer_store_b128(swap16(pk4u(acc[0][b]), pk4u(acc[1][b])), rOut, t * 2048 + 1024 + n * 2, 0, 0);
    }
  }
  {
    WAIT_V(0);
    if (!__all(LANDED_B() == tag)) wait_flags(myp, tag, ~0ull);
    u32x4 v[3][2];
#pragma unroll
    for (int s = 0; s < 3; ++s)
#pragma unroll
      for (int P = 0; P < 2; ++P)
        v[s][P] = __builtin_amdgcn_raw_buffer_load_b128(rW, ptile + b_kq * 32768 + ((b_kq + 1 + s) & 3) * 8192 + P * 1024, 0, SC1);
    int t = b_mt * 128 + wc * 64 + b_kq * 16 + i;
#pragma unroll
    for (int P = 0; P < 2; ++P) {
      f32x4 lo = keep[2 * P], hi = keep[2 * P + 1];
#pragma unroll
      for (int s = 0; s < 3; ++s) {
        const float sg = ((((b_kq + 1 + s) & 3) ^ b_kq) & 2) ? -1.f : 1.f;
        f32x4 a = {blo(v[s][P][0]), bhi(v[s][P][0]), blo(v[s][P][1]), bhi(v[s][P][1])};
        f32x4 b = {blo(v[s][P][2]), bhi(v[s][P][2]), blo(v[s][P][3]), bhi(v[s][P][3])};
        lo += a * sg; hi += b * sg;
      }
      int e = b_nt * 128 + wr * 64 + 32 * P + 8 * g;
      __builtin_amdgcn_raw_buffer_store_b128(pk8(lo, hi) ^ ((b_kq & 2) ? 0x80008000u : 0u), rOut, t * 2048 + e * 2, 0, 0);
    }
    asm volatile(".Lfz_end:");
  }
}

extern "C" void kernel_launch(void* const* d_in, const int* in_sizes, int n_in, void* d_out, int out_size, void* d_ws, size_t ws_size,
                              hipStream_t stream) {
  (void)hipFuncSetAttribute((const void*)fused, hipFuncAttributeMaxDynamicSharedMemorySize, LDS_BYTES);
  fused<<<256, 512, LDS_BYTES, stream>>>((const char*)d_in[0], (const char*)d_in[1], (const char*)d_in[2], (const char*)d_in[3],
                                         (const char*)d_in[4], (const char*)d_in[5], (const char*)d_in[6], (const char*)d_in[7],
                                         (char*)d_out, (char*)d_ws);
}
```
